```python
import torch

def _softmax(x, axis=-1):
    return torch.softmax(x, dim=axis)
def _gelu(x):
    return torch.nn.functional.gelu(x, approximate="tanh")
def _silu(x):
    return torch.nn.functional.silu(x)
def _sigmoid(x):
    return torch.sigmoid(x)
def _layernorm(x, axis=-1, eps=1e-5):
    m = x.mean(dim=axis, keepdim=True)
    v = x.var(dim=axis, keepdim=True, unbiased=False)
    return (x - m) / torch.sqrt(v + eps)
def _scatter_add(tgt, idx, src, axis=0):
    return tgt.clone().index_add_(axis, idx, src)
def _segment_sum(src, seg, n):
    out = torch.zeros((n,) + src.shape[1:], dtype=src.dtype)
    return out.index_add_(0, seg, src)
def _pad(x, pw):
    flat = tuple(v for pair in reversed(pw) for v in pair)
    return torch.nn.functional.pad(x, flat)

kernel_name = 'standalone__weight_only_int_quip__prompt'

def setup_inputs():

    _w = (lambda a: (lambda t: t.to(torch.bfloat16) if t.is_floating_point() else t)(torch.as_tensor(a)))
    B, S, D, F = 1, 2048, 640, 1536
    G = -1 if -1 > 0 else D
    D = (D // G) * G
    ng = D // G
    bits = 8 if 8 > 0 else 4
    qmax = 1 << bits
    qw = torch.randint(0, qmax, (D, F)).to(torch.int32)
    U, _ = (lambda *_a, **_k: torch.linalg.qr(*[torch.as_tensor(_x).float() for _x in _a], **_k))((lambda *s: torch.randn(*s, dtype=torch.bfloat16) * 0.05)(D, D))
    V, _ = (lambda *_a, **_k: torch.linalg.qr(*[torch.as_tensor(_x).float() for _x in _a], **_k))((lambda *s: torch.randn(*s, dtype=torch.bfloat16) * 0.05)(F, F))
    return {
        "x": _w((lambda *s: torch.randn(*s, dtype=torch.bfloat16) * 0.05)(B, S, D)),
        "qw": _w(qw),
        "scales": _w(torch.abs((lambda *s: torch.randn(*s, dtype=torch.bfloat16) * 0.05)(ng, F)) * 0.02 + 0.005),
        "zeros": _w(torch.full((ng, F), qmax / 2.0, dtype=float)),
        "U": _w(U.to(float)),
        "V": _w(V.to(float)),
    }

def reference(x, qw, scales, zeros, U, V):
    B, S, D = x.shape
    ng, F = scales.shape
    G = D // ng
    s = (lambda x, n, axis: torch.repeat_interleave(x, n, dim=axis))(scales, G, axis=0)
    z = (lambda x, n, axis: torch.repeat_interleave(x, n, dim=axis))(zeros, G, axis=0)
    wq = ((lambda x, dt: x.to(dt))(qw, x.dtype) - z) * s
    xr = torch.einsum("bsd,de->bse", x, U)
    h  = torch.einsum("bse,ef->bsf", xr, wq)
    y  = torch.einsum("bsf,fg->bsg", h, V)
    return y
```

```cpp
#include <hip/hip_runtime.h>
#include <stdint.h>

typedef __attribute__((ext_vector_type(8))) __bf16 bf16x8;
typedef __attribute__((ext_vector_type(2))) __bf16 bf16x2;
typedef __attribute__((ext_vector_type(4))) float f32x4;
typedef __attribute__((ext_vector_type(2))) float f32x2;
typedef __attribute__((ext_vector_type(4))) unsigned u32x4;
typedef __attribute__((ext_vector_type(4))) int i32x4;
typedef __attribute__((ext_vector_type(2))) unsigned u32x2;
typedef short v4i16 __attribute__((ext_vector_type(4)));
#define LAS __attribute__((address_space(3)))
typedef LAS char* lptr;

#define DEVI __device__ __forceinline__

constexpr int S_ = 2048, D_ = 640, F_ = 1536;

DEVI void glds(unsigned voff, const void* sbase, unsigned lds) {
    unsigned keep;
    asm volatile("s_mov_b32 %0, m0\n\ts_mov_b32 m0, %3\n\ts_nop 0\n\tglobal_load_lds_dwordx4 %1, %2\n\ts_mov_b32 m0, %0"
                 : "=&s"(keep) : "v"(voff), "s"(sbase), "s"(lds) : "memory");
}
DEVI void glds3(unsigned voff, const void* sbase, unsigned lds) {
    unsigned keep;
    asm volatile("s_mov_b32 %0, m0\n\ts_mov_b32 m0, %3\n\ts_nop 0\n\t"
                 "global_load_lds_dwordx4 %1, %2\n\t"
                 "global_load_lds_dwordx4 %1, %2 offset:1024\n\t"
                 "global_load_lds_dwordx4 %1, %2 offset:2048\n\t"
                 "s_mov_b32 m0, %0"
                 : "=&s"(keep) : "v"(voff), "s"(sbase), "s"(lds) : "memory");
}
DEVI unsigned cvtpk(float lo, float hi) {
    f32x2 v = {lo, hi};
    return __builtin_bit_cast(unsigned, __builtin_convertvector(v, bf16x2));
}
DEVI u32x2 trread(lptr p) {
    return __builtin_bit_cast(u32x2, __builtin_amdgcn_ds_read_tr16_b64_v4i16((LAS v4i16*)p));
}
DEVI f32x4 mfma(u32x4 a, u32x4 b, f32x4 c) {
    return __builtin_amdgcn_mfma_f32_16x16x32_bf16(__builtin_bit_cast(bf16x8, a), __builtin_bit_cast(bf16x8, b), c, 0, 0, 0);
}
#define WAITBAR(N) asm volatile("s_waitcnt vmcnt(" #N ") lgkmcnt(0)\n\ts_barrier" ::: "memory")

constexpr int A_SLOT = 28 * 1024;
constexpr int A_RING = 5;
constexpr int A_TAB = A_SLOT * A_RING;
constexpr int K1_LDS = A_TAB + 12 * 1024;
constexpr int B_SLOT = 36 * 1024;
constexpr int B_RING = 4;

struct ARegs {
    u32x4 a[2];
    u32x4 v[6];
};

DEVI void a_load(ARegs& r, lptr pa0, lptr pa1, lptr pvE, lptr pvO, lptr ptab) {
    i32x4 q00 = *(LAS i32x4*)(pa0), q01 = *(LAS i32x4*)(pa1);
    i32x4 q10 = *(LAS i32x4*)(pa0 + 2048), q11 = *(LAS i32x4*)(pa1 + 2048);
    f32x4 s0 = *(LAS f32x4*)(ptab), z0 = *(LAS f32x4*)(ptab + 16);
    f32x4 s1 = *(LAS f32x4*)(ptab + 128), z1 = *(LAS f32x4*)(ptab + 144);
#pragma unroll
    for (int T = 0; T < 6; ++T) {
        lptr p = ((T & 1) ? pvO : pvE) + 32 * T;
        u32x2 lo = trread(p), hi = trread(p + 16 * 192);
        r.v[T] = u32x4{lo[0], lo[1], hi[0], hi[1]};
    }
    float f[8];
#pragma unroll
    for (int j = 0; j < 4; ++j) {
        f[j] = __builtin_fmaf((float)q00[j], s0[j], z0[j]);
        f[4 + j] = __builtin_fmaf((float)q01[j], s1[j], z1[j]);
    }
    r.a[0] = u32x4{cvtpk(f[0], f[1]), cvtpk(f[2], f[3]), cvtpk(f[4], f[5]), cvtpk(f[6], f[7])};
#pragma unroll
    for (int j = 0; j < 4; ++j) {
        f[j] = __builtin_fmaf((float)q10[j], s0[j], z0[j]);
        f[4 + j] = __builtin_fmaf((float)q11[j], s1[j], z1[j]);
    }
    r.a[1] = u32x4{cvtpk(f[0], f[1]), cvtpk(f[2], f[3]), cvtpk(f[4], f[5]), cvtpk(f[6], f[7])};
}

DEVI void role_w2(lptr sm, unsigned smb, int xcd, int idx, const int* __restrict__ qw, const unsigned short* __restrict__ sc,
                  const unsigned short* __restrict__ zr, const unsigned short* __restrict__ V, char* __restrict__ w2f) {
    const int tid = threadIdx.x, lane = tid & 63;
    const int wid = __builtin_amdgcn_readfirstlane(tid >> 6);
    const int wm = wid & 1, kp = wid >> 1;
    const int g = lane >> 4, r16 = lane & 15;
    const int xi = xcd >> 1, xj = xcd & 1;
    const int ta = idx % 5, tb = idx / 5;
    const int e0 = 320 * (xi & 1) + 64 * ta;
    const int n0 = 768 * xj + 384 * (xi >> 1) + 96 * tb;

    u32x4 tsc, tzr;
    {
        unsigned toff = (tid < 192 ? tid : 191) * 16;
        asm volatile("global_load_dwordx4 %0, %2, %3\n\tglobal_load_dwordx4 %1, %2, %4"
                     : "=&v"(tsc), "=&v"(tzr) : "v"(toff), "s"(sc), "s"(zr) : "memory");
    }
    unsigned va[4], vv[3];
#pragma unroll
    for (int u = 0; u < 4; ++u) {
        int row = 8 * u + (lane >> 3);
        int c = (lane & 7) ^ ((row >> 1) & 7);
        va[u] = (unsigned)((e0 + 32 * wm + row) * (F_ * 4) + kp * 128 + c * 16);
    }
#pragma unroll
    for (int u = 0; u < 3; ++u) {
        int q = (3 * wid + u) * 64 + lane;
        int k = q / 12, c = q % 12;
        c ^= 2 * ((k >> 2) & 1);
        vv[u] = (unsigned)(k * (F_ * 2) + n0 * 2 + c * 16);
    }
    const char* qwb = (const char*)qw;
    const char* vb = (const char*)V;
    auto issue = [&](int st, int slot) {
        const char* pa = qwb + st * 256;
        const char* pv = vb + (size_t)st * (64 * F_ * 2);
        unsigned l = smb + slot * A_SLOT;
#pragma unroll
        for (int u = 0; u < 4; ++u) glds(va[u], pa, l + wid * 4096 + u * 1024);
#pragma unroll
        for (int u = 0; u < 3; ++u) glds(vv[u], pv, l + 16384 + (3 * wid + u) * 1024);
    };
#pragma unroll
    for (int st = 0; st < A_RING; ++st) issue(st, st);

    asm volatile("s_waitcnt vmcnt(35)" : "+v"(tsc), "+v"(tzr)::"memory");
    if (tid < 192) {
        lptr tp = sm + A_TAB + tid * 64;
#pragma unroll
        for (int h = 0; h < 2; ++h) {
            f32x4 s, z;
            s[0] = __uint_as_float(tsc[2 * h] << 16);
            s[1] = __uint_as_float(tsc[2 * h] & 0xffff0000u);
            s[2] = __uint_as_float(tsc[2 * h + 1] << 16);
            s[3] = __uint_as_float(tsc[2 * h + 1] & 0xffff0000u);
            z[0] = -__uint_as_float(tzr[2 * h] << 16) * s[0];
            z[1] = -__uint_as_float(tzr[2 * h] & 0xffff0000u) * s[1];
            z[2] = -__uint_as_float(tzr[2 * h + 1] << 16) * s[2];
            z[3] = -__uint_as_float(tzr[2 * h + 1] & 0xffff0000u) * s[3];
            *(LAS f32x4*)(tp + h * 32) = s;
            *(LAS f32x4*)(tp + h * 32 + 16) = z;
        }
    }
    const int sw = (r16 >> 1) & 7;
    const int oa0 = wid * 4096 + r16 * 128 + ((g ^ sw) << 4);
    const int oa1 = wid * 4096 + r16 * 128 + (((4 + g) ^ sw) << 4);
    const int q4 = r16 >> 2, pp = r16 & 3;
    const int ovrow = 16384 + (32 * kp + 4 * g + q4) * 192 + 8 * pp;
    const int ovE = ovrow + 32 * (g & 1), ovO = ovrow - 32 * (g & 1);
    const int otab = A_TAB + (8 * kp + g) * 32;

    f32x4 acc[2][6];
#pragma unroll
    for (int t = 0; t < 2; ++t)
#pragma unroll
        for (int T = 0; T < 6; ++T) acc[t][T] = f32x4{0.f, 0.f, 0.f, 0.f};

    ARegs R0, R1;
    WAITBAR(28);
    a_load(R0, sm + oa0, sm + oa1, sm + ovE, sm + ovO, sm + otab);

    int slot = 0;
    auto body = [&](int s, ARegs& cur, ARegs& nxt) {
        int rem = 22 - s;
        if (rem >= 3) WAITBAR(21);
        else if (rem == 2) WAITBAR(14);
        else if (rem == 1) WAITBAR(7);
        else WAITBAR(0);
        if (s + A_RING < 24) issue(s + A_RING, slot);
        slot = (slot == A_RING - 1) ? 0 : slot + 1;
        lptr b = sm + slot * A_SLOT;
        int sn = s + 1 < 24 ? s + 1 : 23;
        a_load(nxt, b + oa0, b + oa1, b + ovE, b + ovO, sm + otab + sn * 512);
#pragma unroll
        for (int T = 0; T < 6; ++T) {
            acc[0][T] = mfma(cur.a[0], cur.v[T], acc[0][T]);
            acc[1][T] = mfma(cur.a[1], cur.v[T], acc[1][T]);
        }
    };
#pragma unroll 1
    for (int s = 0; s < 24; s += 2) {
        body(s, R0, R1);
        body(s + 1, R1, R0);
    }
    asm volatile("s_waitcnt lgkmcnt(0)\n\ts_barrier" ::: "memory");
    {
        lptr xw = sm + wid * 6144 + lane * 16;
#pragma unroll
        for (int t = 0; t < 2; ++t)
#pragma unroll
            for (int T = 0; T < 3; ++T) *(LAS f32x4*)(xw + (t * 3 + T) * 1024) = kp ? acc[t][T] : acc[t][3 + T];
    }
    asm volatile("s_waitcnt lgkmcnt(0)\n\ts_barrier" ::: "memory");
    {
        lptr xr = sm + (wid ^ 2) * 6144 + lane * 16;
        const int ks = (e0 >> 5) + wm;
        const int nb = n0 / 96;
#pragma unroll
        for (int T = 0; T < 3; ++T) {
            f32x4 o0 = *(LAS f32x4*)(xr + T * 1024), o1 = *(LAS f32x4*)(xr + (3 + T) * 1024);
            f32x4 m0 = kp ? acc[0][3 + T] : acc[0][T], m1 = kp ? acc[1][3 + T] : acc[1][T];
            m0 += o0;
            m1 += o1;
            u32x4 o = {cvtpk(m0[0], m0[1]), cvtpk(m0[2], m0[3]), cvtpk(m1[0], m1[1]), cvtpk(m1[2], m1[3])};
            int Tp = 3 * kp + T;
            int Tf = 2 * (Tp >> 1) + ((r16 >> 2) & 1);
            int Rf = 4 * (2 * (Tp & 1) + (r16 >> 3)) + (r16 & 3);
            *(u32x4*)(w2f + ((size_t)((nb * 20 + ks) * 6 + Tf)) * 1024 + ((g << 4) | Rf) * 16) = o;
        }
    }
}

struct BRegs {
    u32x4 x[2];
    u32x4 u[10];
};
DEVI void b_load(BRegs& r, lptr px, lptr puE, lptr puO) {
    r.x[0] = *(LAS u32x4*)(px);
    r.x[1] = *(LAS u32x4*)(px + 2048);
#pragma unroll
    for (int T = 0; T < 10; ++T) {
        lptr p = ((T & 1) ? puO : puE) + 32 * T;
        u32x2 lo = trread(p), hi = trread(p + 4 * 320);
        r.u[T] = u32x4{lo[0], lo[1], hi[0], hi[1]};
    }
}

DEVI void role_xr(lptr sm, unsigned smb, int xcd, int idx, const unsigned short* __restrict__ x, const unsigned short* __restrict__ U,
                  char* __restrict__ xrf) {
    const int tid = threadIdx.x, lane = tid & 63;
    const int wid = __builtin_amdgcn_readfirstlane(tid >> 6);
    const int g = lane >> 4, r16 = lane & 15;
    const int xi = xcd >> 1, xj = xcd & 1;
    const int m0 = 512 * xi + 128 * (idx & 3);
    const int e0 = 320 * xj + 160 * (idx >> 2);
    unsigned vx[4], vu[5];
#pragma unroll
    for (int u = 0; u < 4; ++u) {
        int row = 8 * u + (lane >> 3);
        int c = (lane & 7) ^ ((row >> 1) & 7);
        vx[u] = (unsigned)((m0 + 32 * wid + row) * (D_ * 2) + c * 16);
    }
#pragma unroll
    for (int u = 0; u < 5; ++u) {
        int q = (5 * wid + u) * 64 + lane;
        int k = q / 20, c = q % 20;
        c ^= 2 * ((k >> 3) & 1);
        vu[u] = (unsigned)(k * (D_ * 2) + e0 * 2 + c * 16);
    }
    const char* xb = (const char*)x;
    const char* ub = (const char*)U;
    auto issue = [&](int st, int slot) {
        const char* px = xb + st * 128;
        const char* pu = ub + (size_t)st * (64 * D_ * 2);
        unsigned l = smb + slot * B_SLOT;
#pragma unroll
        for (int u = 0; u < 4; ++u) glds(vx[u], px, l + wid * 4096 + u * 1024);
#pragma unroll
        for (int u = 0; u < 5; ++u) glds(vu[u], pu, l + 16384 + (5 * wid + u) * 1024);
    };
#pragma unroll
    for (int st = 0; st < B_RING; ++st) issue(st, st);

    const int sw = (r16 >> 1) & 7;
    const int ox0 = wid * 4096 + r16 * 128 + ((g ^ sw) << 4);
    const int ox1 = wid * 4096 + r16 * 128 + (((4 + g) ^ sw) << 4);
    const int q4 = r16 >> 2, pp = r16 & 3;
    const int ourow = 16384 + (8 * g + q4) * 320 + 8 * pp;
    const int ouE = ourow + 32 * (g & 1), ouO = ourow - 32 * (g & 1);

    f32x4 acc[2][10];
#pragma unroll
    for (int t = 0; t < 2; ++t)
#pragma unroll
        for (int T = 0; T < 10; ++T) acc[t][T] = f32x4{0.f, 0.f, 0.f, 0.f};

    BRegs R0, R1;
    WAITBAR(27);
    b_load(R0, sm + ox0, sm + ouE, sm + ouO);
    int slot = 0;
    auto mm = [&](BRegs& cur) {
#pragma unroll
        for (int T = 0; T < 10; ++T) {
            acc[0][T] = mfma(cur.u[T], cur.x[0], acc[0][T]);
            acc[1][T] = mfma(cur.u[T], cur.x[1], acc[1][T]);
        }
    };
#pragma unroll 1
    for (int s = 0; s < 10; ++s) {
        {
            lptr b = sm + slot * B_SLOT;
            b_load(R1, b + ox1, b + ouE + 32 * 320, b + ouO + 32 * 320);
            mm(R0);
        }
        int rem = 8 - s;
        if (rem >= 2) WAITBAR(18);
        else if (rem == 1) WAITBAR(9);
        else WAITBAR(0);
        if (s + B_RING < 10) issue(s + B_RING, slot);
        slot = (slot == B_RING - 1) ? 0 : slot + 1;
        {
            lptr b = sm + slot * B_SLOT;
            b_load(R0, b + ox0, b + ouE, b + ouO);
            mm(R1);
        }
    }
    {
        const int mb = (m0 >> 5) + wid;
        const int ks0 = e0 >> 5;
#pragma unroll
        for (int kq = 0; kq < 5; ++kq)
#pragma unroll
            for (int t = 0; t < 2; ++t) {
                f32x4 a0 = acc[t][2 * kq], a1 = acc[t][2 * kq + 1];
                u32x4 o = {cvtpk(a0[0], a0[1]), cvtpk(a0[2], a0[3]), cvtpk(a1[0], a1[1]), cvtpk(a1[2], a1[3])};
                *(u32x4*)(xrf + ((size_t)((mb * 20 + ks0 + kq) * 2 + t)) * 1024 + lane * 16) = o;
            }
    }
}

__global__ __launch_bounds__(256, 1) void k1(const unsigned short* __restrict__ x, const int* __restrict__ qw,
                                             const unsigned short* __restrict__ sc, const unsigned short* __restrict__ zr,
                                             const unsigned short* __restrict__ U, const unsigned short* __restrict__ V,
                                             char* __restrict__ xrf, char* __restrict__ w2f) {
    __shared__ __attribute__((aligned(1024))) char smem[K1_LDS];
    lptr sm = (lptr)smem;
    unsigned smb = (unsigned)(uintptr_t)sm;
    const int xcd = blockIdx.x & 7, idx = blockIdx.x >> 3;
    if (idx < 20) role_w2(sm, smb, xcd, idx, qw, sc, zr, V, w2f);
    else role_xr(sm, smb, xcd, idx - 20, x, U, xrf);
}

__global__ __launch_bounds__(256, 1) void k2(const char* __restrict__ xrf, const char* __restrict__ w2f, unsigned short* __restrict__ y) {
    __shared__ __attribute__((aligned(1024))) char smem[120 * 1024];
    lptr sm = (lptr)smem;
    unsigned smb = (unsigned)(uintptr_t)sm;
    const int tid = threadIdx.x, lane = tid & 63;
    const int wid = __builtin_amdgcn_readfirstlane(tid >> 6);
    const int g = lane >> 4, r16 = lane & 15;
    const int xcd = blockIdx.x & 7, idx = blockIdx.x >> 3;
    const int xi = xcd >> 1, xj = xcd & 1;
    const int m0 = 512 * xi + 128 * (idx & 3);
    const int nb = 8 * xj + (idx >> 2);
    const char* pw = w2f + (size_t)nb * (120 * 1024) + wid * 3072;
    const char* px = xrf + (size_t)((m0 >> 5) + wid) * (40 * 1024);
    const unsigned vl = lane * 16;
    u32x4 xf[40];
#pragma unroll
    for (int s = 0; s < 10; ++s) {
        glds3(vl, pw + s * 12288, smb + s * 12288 + wid * 3072);
        asm volatile("global_load_dwordx4 %0, %4, %5\n\t"
                     "global_load_dwordx4 %1, %4, %5 offset:1024\n\t"
                     "global_load_dwordx4 %2, %4, %5 offset:2048\n\t"
                     "global_load_dwordx4 %3, %4, %5 offset:3072"
                     : "=&v"(xf[4 * s]), "=&v"(xf[4 * s + 1]), "=&v"(xf[4 * s + 2]), "=&v"(xf[4 * s + 3])
                     : "v"(vl), "s"(px + s * 4096) : "memory");
    }
    f32x4 acc[2][6];
#pragma unroll
    for (int t = 0; t < 2; ++t)
#pragma unroll
        for (int T = 0; T < 6; ++T) acc[t][T] = f32x4{0.f, 0.f, 0.f, 0.f};
    lptr pl = sm + lane * 16;
    u32x4 wf[2][6];
#define K2WAIT(N, s)                                                                                                   \
    asm volatile("s_waitcnt vmcnt(" #N ")\n\ts_barrier"                                                                \
                 : "+v"(xf[4 * s]), "+v"(xf[4 * s + 1]), "+v"(xf[4 * s + 2]), "+v"(xf[4 * s + 3])::"memory")
    K2WAIT(63, 0);
#pragma unroll
    for (int T = 0; T < 6; ++T) wf[0][T] = *(LAS u32x4*)(pl + T * 1024);
#pragma unroll
    for (int ks = 0; ks < 20; ++ks) {
        if (ks & 1) {
            switch (ks >> 1) {
                case 0: K2WAIT(56, 1); break;
                case 1: K2WAIT(49, 2); break;
                case 2: K2WAIT(42, 3); break;
                case 3: K2WAIT(35, 4); break;
                case 4: K2WAIT(28, 5); break;
                case 5: K2WAIT(21, 6); break;
                case 6: K2WAIT(14, 7); break;
                case 7: K2WAIT(7, 8); break;
                case 8: K2WAIT(0, 9); break;
                default: break;
            }
        }
        if (ks + 1 < 20) {
#pragma unroll
            for (int T = 0; T < 6; ++T) wf[(ks + 1) & 1][T] = *(LAS u32x4*)(pl + ((ks + 1) * 6 + T) * 1024);
        }
#pragma unroll
        for (int T = 0; T < 6; ++T) {
            acc[0][T] = mfma(wf[ks & 1][T], xf[2 * ks], acc[0][T]);
            acc[1][T] = mfma(wf[ks & 1][T], xf[2 * ks + 1], acc[1][T]);
        }
    }
#pragma unroll
    for (int t = 0; t < 2; ++t) {
        unsigned short* yr = y + (size_t)(m0 + 32 * wid + 16 * t + r16) * F_ + nb * 96 + 8 * g;
#pragma unroll
        for (int p = 0; p < 3; ++p) {
            f32x4 a0 = acc[t][2 * p], a1 = acc[t][2 * p + 1];
            u32x4 o = {cvtpk(a0[0], a0[1]), cvtpk(a0[2], a0[3]), cvtpk(a1[0], a1[1]), cvtpk(a1[2], a1[3])};
            *(u32x4*)(yr + 32 * p) = o;
        }
    }
}

extern "C" void kernel_launch(void* const* d_in, const int* in_sizes, int n_in, void* d_out, int out_size, void* d_ws,
                              size_t ws_size, hipStream_t stream) {
    const unsigned short* x = (const unsigned short*)d_in[0];
    const int* qw = (const int*)d_in[1];
    const unsigned short* sc = (const unsigned short*)d_in[2];
    const unsigned short* zr = (const unsigned short*)d_in[3];
    const unsigned short* U = (const unsigned short*)d_in[4];
    const unsigned short* V = (const unsigned short*)d_in[5];
    char* xrf = (char*)d_ws;
    char* w2f = xrf + (size_t)S_ * D_ * 2;
    k1<<<224, 256, 0, stream>>>(x, qw, sc, zr, U, V, xrf, w2f);
    k2<<<256, 256, 0, stream>>>(xrf, w2f, (unsigned short*)d_out);
}
```

```cpp
#include <hip/hip_runtime.h>
#include <stdint.h>

typedef __attribute__((ext_vector_type(8))) __bf16 bf16x8;
typedef __attribute__((ext_vector_type(2))) __bf16 bf16x2;
typedef __attribute__((ext_vector_type(4))) float f32x4;
typedef __attribute__((ext_vector_type(2))) float f32x2;
typedef __attribute__((ext_vector_type(4))) unsigned u32x4;
typedef __attribute__((ext_vector_type(4))) int i32x4;
typedef __attribute__((ext_vector_type(2))) unsigned u32x2;
typedef short v4i16 __attribute__((ext_vector_type(4)));
#define LAS __attribute__((address_space(3)))
typedef LAS char* lptr;

#define DEVI __device__ __forceinline__
#define SB() __builtin_amdgcn_sched_barrier(0)

constexpr int S_ = 2048, D_ = 640, F_ = 1536;

DEVI void glds4(unsigned v0, unsigned v1, unsigned v2, unsigned v3, const void* sbase, unsigned lds) {
    asm volatile("s_mov_b32 m0, %5\n\ts_nop 0\n\t"
                 "global_load_lds_dwordx4 %0, %4\n\t"
                 "global_load_lds_dwordx4 %1, %4 offset:1024\n\t"
                 "global_load_lds_dwordx4 %2, %4 offset:2048\n\t"
                 "global_load_lds_dwordx4 %3, %4 offset:3072"
                 ::"v"(v0), "v"(v1), "v"(v2), "v"(v3), "s"(sbase), "s"(lds) : "memory");
}
DEVI void glds3(unsigned v0, unsigned v1, unsigned v2, const void* sbase, unsigned lds) {
    asm volatile("s_mov_b32 m0, %4\n\ts_nop 0\n\t"
                 "global_load_lds_dwordx4 %0, %3\n\t"
                 "global_load_lds_dwordx4 %1, %3 offset:1024\n\t"
                 "global_load_lds_dwordx4 %2, %3 offset:2048"
                 ::"v"(v0), "v"(v1), "v"(v2), "s"(sbase), "s"(lds) : "memory");
}
DEVI void glds1(unsigned v0, const void* sbase, unsigned lds) {
    asm volatile("s_mov_b32 m0, %2\n\ts_nop 0\n\t"
                 "global_load_lds_dwordx4 %0, %1"
                 ::"v"(v0), "s"(sbase), "s"(lds) : "memory");
}
DEVI unsigned cvtpk(float lo, float hi) {
    f32x2 v = {lo, hi};
    return __builtin_bit_cast(unsigned, __builtin_convertvector(v, bf16x2));
}
DEVI float fma1(float a, float b, float c) {
    float r;
    asm("v_fma_f32 %0, %1, %2, %3" : "=v"(r) : "v"(a), "v"(b), "v"(c));
    return r;
}
DEVI u32x2 trread(lptr p) {
    return __builtin_bit_cast(u32x2, __builtin_amdgcn_ds_read_tr16_b64_v4i16((LAS v4i16*)p));
}
DEVI f32x4 mfma(u32x4 a, u32x4 b, f32x4 c) {
    return __builtin_amdgcn_mfma_f32_16x16x32_bf16(__builtin_bit_cast(bf16x8, a), __builtin_bit_cast(bf16x8, b), c, 0, 0, 0);
}
#define STAMP(k, i)
#define STAMPARG
#define STAMPPASS
#define VMBAR(N) asm volatile("s_waitcnt vmcnt(" #N ")\n\ts_barrier" ::: "memory")
#define LGBAR() asm volatile("s_waitcnt lgkmcnt(0)\n\ts_barrier" ::: "memory")

constexpr int A_SLOT = 28 * 1024;
constexpr int A_RING = 5;
constexpr int A_TAB = A_SLOT * A_RING;
constexpr int K1_LDS = A_TAB + 12 * 1024;
constexpr int B_SLOT = 36 * 1024;
constexpr int B_RING = 4;

struct ARegs {
    u32x4 a[2];
    u32x4 v[6];
};

DEVI u32x4 deq8(i32x4 q0, i32x4 q1, f32x4 s0, f32x4 z0, f32x4 s1, f32x4 z1) {
    float f[8];
#pragma unroll
    for (int j = 0; j < 4; ++j) {
        f[j] = fma1((float)q0[j], s0[j], z0[j]);
        f[4 + j] = fma1((float)q1[j], s1[j], z1[j]);
    }
    return u32x4{cvtpk(f[0], f[1]), cvtpk(f[2], f[3]), cvtpk(f[4], f[5]), cvtpk(f[6], f[7])};
}
DEVI u32x2 deq4(i32x4 q, f32x4 s, f32x4 z) {
    float f[4];
#pragma unroll
    for (int j = 0; j < 4; ++j) f[j] = fma1((float)q[j], s[j], z[j]);
    return u32x2{cvtpk(f[0], f[1]), cvtpk(f[2], f[3])};
}

DEVI void w2_loader(unsigned smb, int lw, int e0, int n0, const int* __restrict__ qw, const unsigned short* __restrict__ V) {
    const int lane = threadIdx.x & 63;
    const int wm = lw & 1, kp = lw >> 1;
    unsigned va[4], vv[3];
#pragma unroll
    for (int u = 0; u < 4; ++u) {
        int row = 8 * u + (lane >> 3);
        int c = (lane & 7) ^ ((row >> 1) & 7);
        va[u] = (unsigned)((e0 + 32 * wm + row) * (F_ * 4) + kp * 128 + c * 16 - u * 1024);
    }
#pragma unroll
    for (int u = 0; u < 3; ++u) {
        int q = (3 * lw + u) * 64 + lane;
        int k = q / 12, c = q % 12;
        c ^= 2 * ((k >> 2) & 1);
        vv[u] = (unsigned)(k * (F_ * 2) + n0 * 2 + c * 16 - u * 1024);
    }
    const char* pa = (const char*)qw;
    const char* pv = (const char*)V;
    unsigned la = smb + lw * 4096;
    const unsigned lvo = 16384 - lw * 4096 + lw * 3072;
    const unsigned lend = la + A_RING * A_SLOT;
    auto issue = [&]() {
        glds4(va[0], va[1], va[2], va[3], pa, la);
        glds3(vv[0], vv[1], vv[2], pv, la + lvo);
        pa += 256;
        pv += 64 * F_ * 2;
        la += A_SLOT;
        if (la == lend) la -= A_RING * A_SLOT;
    };
    issue(); issue(); issue();
    VMBAR(14);
    issue(); issue();
#pragma unroll 1
    for (int s = 0; s < 19; ++s) {
        VMBAR(21);
        issue();
    }
    VMBAR(21);
    VMBAR(14);
    VMBAR(7);
    VMBAR(0);
}

DEVI void w2_compute(lptr sm, int wid, int e0, int n0, const unsigned short* __restrict__ sc, const unsigned short* __restrict__ zr,
                     char* __restrict__ w2f STAMPARG) {
    const int tid = threadIdx.x, lane = tid & 63;
    const int wm = wid & 1, kp = wid >> 1;
    const int g = lane >> 4, r16 = lane & 15;
    STAMP(0, 0);
    if (tid < 192) {
        u32x4 tsc = *(const u32x4*)(sc + tid * 8), tzr = *(const u32x4*)(zr + tid * 8);
        lptr tp = sm + A_TAB + tid * 64;
#pragma unroll
        for (int h = 0; h < 2; ++h) {
            f32x4 s, z;
            s[0] = __uint_as_float(tsc[2 * h] << 16);
            s[1] = __uint_as_float(tsc[2 * h] & 0xffff0000u);
            s[2] = __uint_as_float(tsc[2 * h + 1] << 16);
            s[3] = __uint_as_float(tsc[2 * h + 1] & 0xffff0000u);
            z[0] = -__uint_as_float(tzr[2 * h] << 16) * s[0];
            z[1] = -__uint_as_float(tzr[2 * h] & 0xffff0000u) * s[1];
            z[2] = -__uint_as_float(tzr[2 * h + 1] << 16) * s[2];
            z[3] = -__uint_as_float(tzr[2 * h + 1] & 0xffff0000u) * s[3];
            *(LAS f32x4*)(tp + h * 32) = s;
            *(LAS f32x4*)(tp + h * 32 + 16) = z;
        }
    }
    const int sw = (r16 >> 1) & 7;
    const int oa0 = wid * 4096 + r16 * 128 + ((g ^ sw) << 4);
    const int oa1 = wid * 4096 + r16 * 128 + (((4 + g) ^ sw) << 4);
    const int q4 = r16 >> 2, pp = r16 & 3;
    const int ovrow = 16384 + (32 * kp + 4 * g + q4) * 192 + 8 * pp;
    const int ovE = ovrow + 32 * (g & 1), ovO = ovrow - 32 * (g & 1);
    lptr ptab = sm + A_TAB + (8 * kp + g) * 32;

    f32x4 acc[2][6];
#pragma unroll
    for (int t = 0; t < 2; ++t)
#pragma unroll
        for (int T = 0; T < 6; ++T) acc[t][T] = f32x4{0.f, 0.f, 0.f, 0.f};

    ARegs R0, R1;
    auto vload = [&](ARegs& r, lptr pvE, lptr pvO, int T) {
        lptr p = ((T & 1) ? pvO : pvE) + 32 * T;
        u32x2 lo = trread(p), hi = trread(p + 16 * 192);
        r.v[T] = u32x4{lo[0], lo[1], hi[0], hi[1]};
    };
    STAMP(0, 1);
    LGBAR();
    STAMP(0, 2);
    {
        i32x4 q00 = *(LAS i32x4*)(sm + oa0), q01 = *(LAS i32x4*)(sm + oa1);
        i32x4 q10 = *(LAS i32x4*)(sm + oa0 + 2048), q11 = *(LAS i32x4*)(sm + oa1 + 2048);
        f32x4 s0 = *(LAS f32x4*)(ptab), z0 = *(LAS f32x4*)(ptab + 16);
        f32x4 s1 = *(LAS f32x4*)(ptab + 128), z1 = *(LAS f32x4*)(ptab + 144);
#pragma unroll
        for (int T = 0; T < 6; ++T) vload(R0, sm + ovE, sm + ovO, T);
        R0.a[0] = deq8(q00, q01, s0, z0, s1, z1);
        R0.a[1] = deq8(q10, q11, s0, z0, s1, z1);
    }
    int slotoff = 0;
    auto body = [&](ARegs& cur, ARegs& nxt) {
        slotoff = (slotoff == (A_RING - 1) * A_SLOT) ? 0 : slotoff + A_SLOT;
        ptab += 512;
        lptr b = sm + slotoff;
        lptr pa0 = b + oa0, pa1 = b + oa1, pvE = b + ovE, pvO = b + ovO;
        LGBAR();
        SB();
        i32x4 q00 = *(LAS i32x4*)(pa0), q01 = *(LAS i32x4*)(pa1);
        f32x4 s0 = *(LAS f32x4*)(ptab), z0 = *(LAS f32x4*)(ptab + 16);
        acc[0][0] = mfma(cur.a[0], cur.v[0], acc[0][0]);
        f32x4 s1 = *(LAS f32x4*)(ptab + 128), z1 = *(LAS f32x4*)(ptab + 144);
        acc[1][0] = mfma(cur.a[1], cur.v[0], acc[1][0]);
        i32x4 q10 = *(LAS i32x4*)(pa0 + 2048), q11 = *(LAS i32x4*)(pa1 + 2048);
        SB();
        acc[0][1] = mfma(cur.a[0], cur.v[1], acc[0][1]);
        vload(nxt, pvE, pvO, 0);
        SB();
        acc[1][1] = mfma(cur.a[1], cur.v[1], acc[1][1]);
        vload(nxt, pvE, pvO, 1);
        SB();
        acc[0][2] = mfma(cur.a[0], cur.v[2], acc[0][2]);
        vload(nxt, pvE, pvO, 2);
        SB();
        acc[1][2] = mfma(cur.a[1], cur.v[2], acc[1][2]);
        vload(nxt, pvE, pvO, 3);
        SB();
        acc[0][3] = mfma(cur.a[0], cur.v[3], acc[0][3]);
        u32x2 a00 = deq4(q00, s0, z0);
        SB();
        acc[1][3] = mfma(cur.a[1], cur.v[3], acc[1][3]);
        vload(nxt, pvE, pvO, 4);
        u32x2 a01 = deq4(q01, s1, z1);
        SB();
        acc[0][4] = mfma(cur.a[0], cur.v[4], acc[0][4]);
        vload(nxt, pvE, pvO, 5);
        u32x2 a10 = deq4(q10, s0, z0);
        SB();
        acc[1][4] = mfma(cur.a[1], cur.v[4], acc[1][4]);
        u32x2 a11 = deq4(q11, s1, z1);
        SB();
        acc[0][5] = mfma(cur.a[0], cur.v[5], acc[0][5]);
        acc[1][5] = mfma(cur.a[1], cur.v[5], acc[1][5]);
        nxt.a[0] = u32x4{a00[0], a00[1], a01[0], a01[1]};
        nxt.a[1] = u32x4{a10[0], a10[1], a11[0], a11[1]};
        SB();
    };
#pragma unroll 1
    for (int s = 0; s < 22; s += 2) {
        if (s % 4 == 0) STAMP(0, 3 + s / 4);
        body(R0, R1);
        body(R1, R0);
    }
    body(R0, R1);
#pragma unroll
    for (int T = 0; T < 6; ++T) {
        acc[0][T] = mfma(R1.a[0], R1.v[T], acc[0][T]);
        acc[1][T] = mfma(R1.a[1], R1.v[T], acc[1][T]);
    }
    STAMP(0, 9);
    {
        lptr xw = sm + wid * 6144 + lane * 16;
#pragma unroll
        for (int t = 0; t < 2; ++t)
#pragma unroll
            for (int T = 0; T < 3; ++T) *(LAS f32x4*)(xw + (t * 3 + T) * 1024) = kp ? acc[t][T] : acc[t][3 + T];
    }
    LGBAR();
    {
        lptr xr = sm + (wid ^ 2) * 6144 + lane * 16;
        const int ks = (e0 >> 5) + wm;
        const int nb = n0 / 96;
#pragma unroll
        for (int T = 0; T < 3; ++T) {
            f32x4 o0 = *(LAS f32x4*)(xr + T * 1024), o1 = *(LAS f32x4*)(xr + (3 + T) * 1024);
            f32x4 m0 = kp ? acc[0][3 + T] : acc[0][T], m1 = kp ? acc[1][3 + T] : acc[1][T];
            m0 += o0;
            m1 += o1;
            u32x4 o = {cvtpk(m0[0], m0[1]), cvtpk(m0[2], m0[3]), cvtpk(m1[0], m1[1]), cvtpk(m1[2], m1[3])};
            int Tp = 3 * kp + T;
            int Tf = 2 * (Tp >> 1) + ((r16 >> 2) & 1);
            int Rf = 4 * (2 * (Tp & 1) + (r16 >> 3)) + (r16 & 3);
            *(u32x4*)(w2f + ((size_t)((nb * 20 + ks) * 6 + Tf)) * 1024 + ((g << 4) | Rf) * 16) = o;
        }
    }
    STAMP(0, 10);
}

DEVI void xr_loader(unsigned smb, int lw, int m0, int e0, const unsigned short* __restrict__ x, const unsigned short* __restrict__ U) {
    const int lane = threadIdx.x & 63;
    unsigned vx[4], vu[5];
#pragma unroll
    for (int u = 0; u < 4; ++u) {
        int row = 8 * u + (lane >> 3);
        int c = (lane & 7) ^ ((row >> 1) & 7);
        vx[u] = (unsigned)((m0 + 32 * lw + row) * (D_ * 2) + c * 16 - u * 1024);
    }
#pragma unroll
    for (int u = 0; u < 5; ++u) {
        int q = (5 * lw + u) * 64 + lane;
        int k = q / 20, c = q % 20;
        c ^= 2 * ((k >> 3) & 1);
        vu[u] = (unsigned)(k * (D_ * 2) + e0 * 2 + c * 16 - (u & 3) * 1024);
    }
    const char* px = (const char*)x;
    const char* pu = (const char*)U;
    unsigned la = smb + lw * 4096;
    const unsigned luo = 16384 - lw * 4096 + lw * 5120;
    const unsigned lend = la + B_RING * B_SLOT;
    auto issue = [&]() {
        glds4(vx[0], vx[1], vx[2], vx[3], px, la);
        glds4(vu[0], vu[1], vu[2], vu[3], pu, la + luo);
        glds1(vu[4], pu, la + luo + 4096);
        px += 128;
        pu += 64 * D_ * 2;
        la += B_SLOT;
        if (la == lend) la -= B_RING * B_SLOT;
    };
    issue(); issue();
    VMBAR(9);
    issue(); issue();
#pragma unroll 1
    for (int s = 0; s < 6; ++s) {
        VMBAR(18);
        issue();
    }
    VMBAR(18);
    VMBAR(9);
    VMBAR(0);
}

struct BRegs {
    u32x4 x[2];
    u32x4 u[10];
};

DEVI void xr_compute(lptr sm, int wid, int m0, int e0, char* __restrict__ xrf STAMPARG) {
    const int tid = threadIdx.x, lane = tid & 63;
    const int g = lane >> 4, r16 = lane & 15;
    STAMP(0, 0);
    const int sw = (r16 >> 1) & 7;
    const int ox0 = wid * 4096 + r16 * 128 + ((g ^ sw) << 4);
    const int ox1 = wid * 4096 + r16 * 128 + (((4 + g) ^ sw) << 4);
    const int q4 = r16 >> 2, pp = r16 & 3;
    const int ourow = 16384 + (8 * g + q4) * 320 + 8 * pp;
    const int ouE = ourow + 32 * (g & 1), ouO = ourow - 32 * (g & 1);

    f32x4 acc[2][10];
#pragma unroll
    for (int t = 0; t < 2; ++t)
#pragma unroll
        for (int T = 0; T < 10; ++T) acc[t][T] = f32x4{0.f, 0.f, 0.f, 0.f};

    BRegs R0, R1;
    auto uload = [&](BRegs& r, lptr puE, lptr puO, int T) {
        lptr p = ((T & 1) ? puO : puE) + 32 * T;
        u32x2 lo = trread(p), hi = trread(p + 4 * 320);
        r.u[T] = u32x4{lo[0], lo[1], hi[0], hi[1]};
    };
    auto step = [&](BRegs& cur, BRegs& nxt, lptr px, lptr puE, lptr puO) {
        SB();
        nxt.x[0] = *(LAS u32x4*)(px);
        nxt.x[1] = *(LAS u32x4*)(px + 2048);
#pragma unroll
        for (int T = 0; T < 10; ++T) {
            acc[0][T] = mfma(cur.u[T], cur.x[0], acc[0][T]);
            uload(nxt, puE, puO, T);
            acc[1][T] = mfma(cur.u[T], cur.x[1], acc[1][T]);
            SB();
        }
    };
    STAMP(0, 1);
    LGBAR();
    STAMP(0, 2);
    R0.x[0] = *(LAS u32x4*)(sm + ox0);
    R0.x[1] = *(LAS u32x4*)(sm + ox0 + 2048);
#pragma unroll
    for (int T = 0; T < 10; ++T) uload(R0, sm + ouE, sm + ouO, T);
    int slotoff = 0;
#pragma unroll 1
    for (int s = 0; s < 9; ++s) {
        if (s % 2 == 0) STAMP(0, 3 + s / 2);
        lptr b = sm + slotoff;
        step(R0, R1, b + ox1, b + ouE + 32 * 320, b + ouO + 32 * 320);
        slotoff = (slotoff == (B_RING - 1) * B_SLOT) ? 0 : slotoff + B_SLOT;
        b = sm + slotoff;
        LGBAR();
        step(R1, R0, b + ox0, b + ouE, b + ouO);
    }
    {
        lptr b = sm + slotoff;
        step(R0, R1, b + ox1, b + ouE + 32 * 320, b + ouO + 32 * 320);
#pragma unroll
        for (int T = 0; T < 10; ++T) {
            acc[0][T] = mfma(R1.u[T], R1.x[0], acc[0][T]);
            acc[1][T] = mfma(R1.u[T], R1.x[1], acc[1][T]);
        }
    }
    STAMP(0, 9);
    {
        const int mb = (m0 >> 5) + wid;
        const int ks0 = e0 >> 5;
#pragma unroll
        for (int kq = 0; kq < 5; ++kq)
#pragma unroll
            for (int t = 0; t < 2; ++t) {
                f32x4 a0 = acc[t][2 * kq], a1 = acc[t][2 * kq + 1];
                u32x4 o = {cvtpk(a0[0], a0[1]), cvtpk(a0[2], a0[3]), cvtpk(a1[0], a1[1]), cvtpk(a1[2], a1[3])};
                *(u32x4*)(xrf + ((size_t)((mb * 20 + ks0 + kq) * 2 + t)) * 1024 + lane * 16) = o;
            }
    }
    STAMP(0, 10);
}

__global__ __launch_bounds__(512, 2) void k1(const unsigned short* __restrict__ x, const int* __restrict__ qw,
                                             const unsigned short* __restrict__ sc, const unsigned short* __restrict__ zr,
                                             const unsigned short* __restrict__ U, const unsigned short* __restrict__ V,
                                             char* __restrict__ xrf, char* __restrict__ w2f STAMPARG) {
    __shared__ __attribute__((aligned(1024))) char smem[K1_LDS];
    lptr sm = (lptr)smem;
    unsigned smb = (unsigned)(uintptr_t)sm;
    const int xcd = blockIdx.x & 7, idx = blockIdx.x >> 3;
    const int wid = __builtin_amdgcn_readfirstlane(threadIdx.x >> 6);
    const int xi = xcd >> 1, xj = xcd & 1;
    if (idx < 20) {
        const int ta = idx % 5, tb = idx / 5;
        const int e0 = 320 * (xi & 1) + 64 * ta;
        const int n0 = 768 * xj + 384 * (xi >> 1) + 96 * tb;
        if (wid >= 4) w2_loader(smb, wid - 4, e0, n0, qw, V);
        else w2_compute(sm, wid, e0, n0, sc, zr, w2f STAMPPASS);
    } else {
        const int i2 = idx - 20;
        const int m0 = 512 * xi + 128 * (i2 & 3);
        const int e0 = 320 * xj + 160 * (i2 >> 2);
        if (wid >= 4) xr_loader(smb, wid - 4, m0, e0, x, U);
        else xr_compute(sm, wid, m0, e0, xrf STAMPPASS);
    }
}

constexpr int C_SLOT = 28 * 1024;
constexpr int C_RING = 5;
struct CRegs {
    u32x4 x[2];
    u32x4 w[6];
};
__global__ __launch_bounds__(512, 2) void k2(const char* __restrict__ xrf, const char* __restrict__ w2f, unsigned short* __restrict__ y STAMPARG) {
    __shared__ __attribute__((aligned(1024))) char smem[C_SLOT * C_RING];
    lptr sm = (lptr)smem;
    unsigned smb = (unsigned)(uintptr_t)sm;
    const int tid = threadIdx.x, lane = tid & 63;
    const int wid = __builtin_amdgcn_readfirstlane(tid >> 6);
    const int xcd = blockIdx.x & 7, idx = blockIdx.x >> 3;
    const int xi = xcd >> 1, xj = xcd & 1;
    const int m0 = 512 * xi + 128 * (idx & 3);
    const int nb = 8 * xj + (idx >> 2);
    if (wid >= 4) {
        const int lw = wid - 4;
        const char* pw = w2f + (size_t)nb * (120 * 1024) + lw * 3072;
        const char* px = xrf + (size_t)((m0 >> 5) + lw) * (40 * 1024);
        const unsigned vl = lane * 16;
        unsigned la = smb + lw * 4096;
        const unsigned lwo = 16384 - lw * 4096 + lw * 3072;
        auto issue = [&]() {
            glds4(vl, vl, vl, vl, px, la);
            glds3(vl, vl, vl, pw, la + lwo);
            px += 4096;
            pw += 12288;
            la += C_SLOT;
        };
        issue(); issue(); issue();
        VMBAR(14);
        issue(); issue();
        la -= C_RING * C_SLOT;
#pragma unroll 1
        for (int s = 0; s < 5; ++s) {
            VMBAR(21);
            issue();
        }
        VMBAR(21);
        VMBAR(14);
        VMBAR(7);
        VMBAR(0);
        return;
    }
    STAMP(1, 0);
    const int g = lane >> 4, r16 = lane & 15;
    f32x4 acc[2][6];
#pragma unroll
    for (int t = 0; t < 2; ++t)
#pragma unroll
        for (int T = 0; T < 6; ++T) acc[t][T] = f32x4{0.f, 0.f, 0.f, 0.f};
    const int ox = wid * 4096 + lane * 16, ow = 16384 + lane * 16;
    CRegs R0, R1;
    auto step = [&](CRegs& cur, CRegs& nxt, lptr px, lptr pw) {
        SB();
        nxt.x[0] = *(LAS u32x4*)(px);
        nxt.x[1] = *(LAS u32x4*)(px + 1024);
#pragma unroll
        for (int T = 0; T < 6; ++T) {
            acc[0][T] = mfma(cur.w[T], cur.x[0], acc[0][T]);
            nxt.w[T] = *(LAS u32x4*)(pw + T * 1024);
            acc[1][T] = mfma(cur.w[T], cur.x[1], acc[1][T]);
            SB();
        }
    };
    STAMP(1, 1);
    LGBAR();
    STAMP(1, 2);
    R0.x[0] = *(LAS u32x4*)(sm + ox);
    R0.x[1] = *(LAS u32x4*)(sm + ox + 1024);
#pragma unroll
    for (int T = 0; T < 6; ++T) R0.w[T] = *(LAS u32x4*)(sm + ow + T * 1024);
    int slotoff = 0;
#pragma unroll 1
    for (int s = 0; s < 9; ++s) {
        if (s % 2 == 0) STAMP(1, 3 + s / 2);
        lptr b = sm + slotoff;
        step(R0, R1, b + ox + 2048, b + ow + 6144);
        slotoff = (slotoff == (C_RING - 1) * C_SLOT) ? 0 : slotoff + C_SLOT;
        b = sm + slotoff;
        LGBAR();
        step(R1, R0, b + ox, b + ow);
    }
    {
        lptr b = sm + slotoff;
        step(R0, R1, b + ox + 2048, b + ow + 6144);
#pragma unroll
        for (int T = 0; T < 6; ++T) {
            acc[0][T] = mfma(R1.w[T], R1.x[0], acc[0][T]);
            acc[1][T] = mfma(R1.w[T], R1.x[1], acc[1][T]);
        }
    }
    STAMP(1, 9);
#pragma unroll
    for (int t = 0; t < 2; ++t) {
        unsigned short* yr = y + (size_t)(m0 + 32 * wid + 16 * t + r16) * F_ + nb * 96 + 8 * g;
#pragma unroll
        for (int p = 0; p < 3; ++p) {
            f32x4 a0 = acc[t][2 * p], a1 = acc[t][2 * p + 1];
            u32x4 o = {cvtpk(a0[0], a0[1]), cvtpk(a0[2], a0[3]), cvtpk(a1[0], a1[1]), cvtpk(a1[2], a1[3])};
            *(u32x4*)(yr + 32 * p) = o;
        }
    }
    STAMP(1, 10);
}

extern "C" void kernel_launch(void* const* d_in, const int* in_sizes, int n_in, void* d_out, int out_size, void* d_ws,
                              size_t ws_size, hipStream_t stream) {
    const unsigned short* x = (const unsigned short*)d_in[0];
    const int* qw = (const int*)d_in[1];
    const unsigned short* sc = (const unsigned short*)d_in[2];
    const unsigned short* zr = (const unsigned short*)d_in[3];
    const unsigned short* U = (const unsigned short*)d_in[4];
    const unsigned short* V = (const unsigned short*)d_in[5];
    char* xrf = (char*)d_ws;
    char* w2f = xrf + (size_t)S_ * D_ * 2;
    k1<<<224, 512, 0, stream>>>(x, qw, sc, zr, U, V, xrf, w2f STAMPPASS);
    k2<<<256, 512, 0, stream>>>(xrf, w2f, (unsigned short*)d_out STAMPPASS);
}
```

```cpp
#define STAMPS
#define PROBE 0
#include <hip/hip_runtime.h>
#include <stdint.h>

typedef __attribute__((ext_vector_type(8))) __bf16 bf16x8;
typedef __attribute__((ext_vector_type(2))) __bf16 bf16x2;
typedef __attribute__((ext_vector_type(4))) float f32x4;
typedef __attribute__((ext_vector_type(2))) float f32x2;
typedef __attribute__((ext_vector_type(4))) unsigned u32x4;
typedef __attribute__((ext_vector_type(4))) int i32x4;
typedef __attribute__((ext_vector_type(2))) unsigned u32x2;
typedef short v4i16 __attribute__((ext_vector_type(4)));
#define LAS __attribute__((address_space(3)))
typedef LAS char* lptr;

#define DEVI __device__ __forceinline__
#define SB() __builtin_amdgcn_sched_barrier(0)

constexpr int S_ = 2048, D_ = 640, F_ = 1536;

DEVI void glds4(unsigned v0, unsigned v1, unsigned v2, unsigned v3, const void* sbase, unsigned lds) {
    asm volatile("s_mov_b32 m0, %5\n\ts_nop 0\n\t"
                 "global_load_lds_dwordx4 %0, %4\n\t"
                 "global_load_lds_dwordx4 %1, %4 offset:1024\n\t"
                 "global_load_lds_dwordx4 %2, %4 offset:2048\n\t"
                 "global_load_lds_dwordx4 %3, %4 offset:3072"
                 ::"v"(v0), "v"(v1), "v"(v2), "v"(v3), "s"(sbase), "s"(lds) : "memory");
}
DEVI void glds3(unsigned v0, unsigned v1, unsigned v2, const void* sbase, unsigned lds) {
    asm volatile("s_mov_b32 m0, %4\n\ts_nop 0\n\t"
                 "global_load_lds_dwordx4 %0, %3\n\t"
                 "global_load_lds_dwordx4 %1, %3 offset:1024\n\t"
                 "global_load_lds_dwordx4 %2, %3 offset:2048"
                 ::"v"(v0), "v"(v1), "v"(v2), "s"(sbase), "s"(lds) : "memory");
}
DEVI void glds1(unsigned v0, const void* sbase, unsigned lds) {
    asm volatile("s_mov_b32 m0, %2\n\ts_nop 0\n\t"
                 "global_load_lds_dwordx4 %0, %1"
                 ::"v"(v0), "s"(sbase), "s"(lds) : "memory");
}
DEVI unsigned cvtpk(float lo, float hi) {
    f32x2 v = {lo, hi};
    return __builtin_bit_cast(unsigned, __builtin_convertvector(v, bf16x2));
}
DEVI float fma1(float a, float b, float c) {
    float r;
    asm("v_fma_f32 %0, %1, %2, %3" : "=v"(r) : "v"(a), "v"(b), "v"(c));
    return r;
}
DEVI u32x2 trread(lptr p) {
    return __builtin_bit_cast(u32x2, __builtin_amdgcn_ds_read_tr16_b64_v4i16((LAS v4i16*)p));
}
DEVI f32x4 mfma(u32x4 a, u32x4 b, f32x4 c) {
    return __builtin_amdgcn_mfma_f32_16x16x32_bf16(__builtin_bit_cast(bf16x8, a), __builtin_bit_cast(bf16x8, b), c, 0, 0, 0);
}
#define STAMP(k, i) do { if (threadIdx.x == 0) { unsigned long long t_ = __builtin_amdgcn_s_memrealtime(); stamps[((k) * 256 + blockIdx.x) * 16 + (i)] = t_; stamps[((2 + k) * 256 + blockIdx.x) * 16 + (i)] = __builtin_amdgcn_s_memtime(); } } while (0)
#define STAMPARG , unsigned long long* __restrict__ stamps
#define STAMPPASS , stamps
#define VMBAR(N) asm volatile("s_waitcnt vmcnt(" #N ")\n\ts_barrier" ::: "memory")
#define LGBAR() asm volatile("s_waitcnt lgkmcnt(0)\n\ts_barrier" ::: "memory")

constexpr int A_SLOT = 28 * 1024;
constexpr int A_RING = 5;
constexpr int A_TAB = A_SLOT * A_RING;
constexpr int K1_LDS = A_TAB + 12 * 1024;
constexpr int B_SLOT = 36 * 1024;
constexpr int B_RING = 4;

struct ARegs {
    u32x4 a[2];
    u32x4 v[6];
};

DEVI u32x4 deq8(i32x4 q0, i32x4 q1, f32x4 s0, f32x4 z0, f32x4 s1, f32x4 z1) {
    float f[8];
#pragma unroll
    for (int j = 0; j < 4; ++j) {
        f[j] = fma1((float)q0[j], s0[j], z0[j]);
        f[4 + j] = fma1((float)q1[j], s1[j], z1[j]);
    }
    return u32x4{cvtpk(f[0], f[1]), cvtpk(f[2], f[3]), cvtpk(f[4], f[5]), cvtpk(f[6], f[7])};
}
DEVI u32x2 deq4(i32x4 q, f32x4 s, f32x4 z) {
    float f[4];
#pragma unroll
    for (int j = 0; j < 4; ++j) f[j] = fma1((float)q[j], s[j], z[j]);
    return u32x2{cvtpk(f[0], f[1]), cvtpk(f[2], f[3])};
}

DEVI void w2_loader(unsigned smb, int lw, int e0, int n0, const int* __restrict__ qw, const unsigned short* __restrict__ V) {
    const int lane = threadIdx.x & 63;
    const int wm = lw & 1, kp = lw >> 1;
    unsigned va[4], vv[3];
#pragma unroll
    for (int u = 0; u < 4; ++u) {
        int row = 8 * u + (lane >> 3);
        int c = (lane & 7) ^ ((row >> 1) & 7);
        va[u] = (unsigned)((e0 + 32 * wm + row) * (F_ * 4) + kp * 128 + c * 16 - u * 1024);
    }
#pragma unroll
    for (int u = 0; u < 3; ++u) {
        int q = (3 * lw + u) * 64 + lane;
        int k = q / 12, c = q % 12;
        c ^= 2 * ((k >> 2) & 1);
        vv[u] = (unsigned)(k * (F_ * 2) + n0 * 2 + c * 16 - u * 1024);
    }
    const char* pa = (const char*)qw;
    const char* pv = (const char*)V;
    unsigned la = smb + lw * 4096;
    const unsigned lvo = 16384 - lw * 4096 + lw * 3072;
    const unsigned lend = la + A_RING * A_SLOT;
    auto issue = [&]() {
        glds4(va[0], va[1], va[2], va[3], pa, la);
        glds3(vv[0], vv[1], vv[2], pv, la + lvo);
        pa += 256;
        pv += 64 * F_ * 2;
        la += A_SLOT;
        if (la == lend) la -= A_RING * A_SLOT;
    };
    issue(); issue(); issue();
    VMBAR(14);
    issue(); issue();
#pragma unroll 1
    for (int s = 0; s < 19; ++s) {
        VMBAR(21);
        issue();
    }
    VMBAR(21);
    VMBAR(14);
    VMBAR(7);
    VMBAR(0);
}

DEVI void w2_compute(lptr sm, int wid, int e0, int n0, const unsigned short* __restrict__ sc, const unsigned short* __restrict__ zr,
                     char* __restrict__ w2f STAMPARG) {
    const int tid = threadIdx.x, lane = tid & 63;
    const int wm = wid & 1, kp = wid >> 1;
    const int g = lane >> 4, r16 = lane & 15;
    STAMP(0, 0);
    if (tid < 192) {
        u32x4 tsc = *(const u32x4*)(sc + tid * 8), tzr = *(const u32x4*)(zr + tid * 8);
        lptr tp = sm + A_TAB + tid * 64;
#pragma unroll
        for (int h = 0; h < 2; ++h) {
            f32x4 s, z;
            s[0] = __uint_as_float(tsc[2 * h] << 16);
            s[1] = __uint_as_float(tsc[2 * h] & 0xffff0000u);
            s[2] = __uint_as_float(tsc[2 * h + 1] << 16);
            s[3] = __uint_as_float(tsc[2 * h + 1] & 0xffff0000u);
            z[0] = -__uint_as_float(tzr[2 * h] << 16) * s[0];
            z[1] = -__uint_as_float(tzr[2 * h] & 0xffff0000u) * s[1];
            z[2] = -__uint_as_float(tzr[2 * h + 1] << 16) * s[2];
            z[3] = -__uint_as_float(tzr[2 * h + 1] & 0xffff0000u) * s[3];
            *(LAS f32x4*)(tp + h * 32) = s;
            *(LAS f32x4*)(tp + h * 32 + 16) = z;
        }
    }
    const int sw = (r16 >> 1) & 7;
    const int oa0 = wid * 4096 + r16 * 128 + ((g ^ sw) << 4);
    const int oa1 = wid * 4096 + r16 * 128 + (((4 + g) ^ sw) << 4);
    const int q4 = r16 >> 2, pp = r16 & 3;
    const int ovrow = 16384 + (32 * kp + 4 * g + q4) * 192 + 8 * pp;
    const int ovE = ovrow + 32 * (g & 1), ovO = ovrow - 32 * (g & 1);
    lptr ptab = sm + A_TAB + (8 * kp + g) * 32;

    f32x4 acc[2][6];
#pragma unroll
    for (int t = 0; t < 2; ++t)
#pragma unroll
        for (int T = 0; T < 6; ++T) acc[t][T] = f32x4{0.f, 0.f, 0.f, 0.f};

    ARegs R0, R1;
    auto vload = [&](ARegs& r, lptr pvE, lptr pvO, int T) {
        lptr p = ((T & 1) ? pvO : pvE) + 32 * T;
        u32x2 lo = trread(p), hi = trread(p + 16 * 192);
        r.v[T] = u32x4{lo[0], lo[1], hi[0], hi[1]};
    };
    STAMP(0, 1);
    LGBAR();
    STAMP(0, 2);
    {
        i32x4 q00 = *(LAS i32x4*)(sm + oa0), q01 = *(LAS i32x4*)(sm + oa1);
        i32x4 q10 = *(LAS i32x4*)(sm + oa0 + 2048), q11 = *(LAS i32x4*)(sm + oa1 + 2048);
        f32x4 s0 = *(LAS f32x4*)(ptab), z0 = *(LAS f32x4*)(ptab + 16);
        f32x4 s1 = *(LAS f32x4*)(ptab + 128), z1 = *(LAS f32x4*)(ptab + 144);
#pragma unroll
        for (int T = 0; T < 6; ++T) vload(R0, sm + ovE, sm + ovO, T);
        R0.a[0] = deq8(q00, q01, s0, z0, s1, z1);
        R0.a[1] = deq8(q10, q11, s0, z0, s1, z1);
    }
    int slotoff = 0;
    auto body = [&](ARegs& cur, ARegs& nxt) {
        slotoff = (slotoff == (A_RING - 1) * A_SLOT) ? 0 : slotoff + A_SLOT;
        ptab += 512;
        lptr b = sm + slotoff;
        lptr pa0 = b + oa0, pa1 = b + oa1, pvE = b + ovE, pvO = b + ovO;
        LGBAR();
        SB();
        i32x4 q00 = *(LAS i32x4*)(pa0), q01 = *(LAS i32x4*)(pa1);
        f32x4 s0 = *(LAS f32x4*)(ptab), z0 = *(LAS f32x4*)(ptab + 16);
        acc[0][0] = mfma(cur.a[0], cur.v[0], acc[0][0]);
        f32x4 s1 = *(LAS f32x4*)(ptab + 128), z1 = *(LAS f32x4*)(ptab + 144);
        acc[1][0] = mfma(cur.a[1], cur.v[0], acc[1][0]);
        i32x4 q10 = *(LAS i32x4*)(pa0 + 2048), q11 = *(LAS i32x4*)(pa1 + 2048);
        SB();
        acc[0][1] = mfma(cur.a[0], cur.v[1], acc[0][1]);
        vload(nxt, pvE, pvO, 0);
        SB();
        acc[1][1] = mfma(cur.a[1], cur.v[1], acc[1][1]);
        vload(nxt, pvE, pvO, 1);
        SB();
        acc[0][2] = mfma(cur.a[0], cur.v[2], acc[0][2]);
        vload(nxt, pvE, pvO, 2);
        SB();
        acc[1][2] = mfma(cur.a[1], cur.v[2], acc[1][2]);
        vload(nxt, pvE, pvO, 3);
        SB();
        acc[0][3] = mfma(cur.a[0], cur.v[3], acc[0][3]);
        u32x2 a00 = deq4(q00, s0, z0);
        SB();
        acc[1][3] = mfma(cur.a[1], cur.v[3], acc[1][3]);
        vload(nxt, pvE, pvO, 4);
        u32x2 a01 = deq4(q01, s1, z1);
        SB();
        acc[0][4] = mfma(cur.a[0], cur.v[4], acc[0][4]);
        vload(nxt, pvE, pvO, 5);
        u32x2 a10 = deq4(q10, s0, z0);
        SB();
        acc[1][4] = mfma(cur.a[1], cur.v[4], acc[1][4]);
        u32x2 a11 = deq4(q11, s1, z1);
        SB();
        acc[0][5] = mfma(cur.a[0], cur.v[5], acc[0][5]);
        acc[1][5] = mfma(cur.a[1], cur.v[5], acc[1][5]);
        nxt.a[0] = u32x4{a00[0], a00[1], a01[0], a01[1]};
        nxt.a[1] = u32x4{a10[0], a10[1], a11[0], a11[1]};
        SB();
    };
#pragma unroll 1
    for (int s = 0; s < 22; s += 2) {
        if (s % 4 == 0) STAMP(0, 3 + s / 4);
        body(R0, R1);
        body(R1, R0);
    }
    body(R0, R1);
#pragma unroll
    for (int T = 0; T < 6; ++T) {
        acc[0][T] = mfma(R1.a[0], R1.v[T], acc[0][T]);
        acc[1][T] = mfma(R1.a[1], R1.v[T], acc[1][T]);
    }
    STAMP(0, 9);
    {
        lptr xw = sm + wid * 6144 + lane * 16;
#pragma unroll
        for (int t = 0; t < 2; ++t)
#pragma unroll
            for (int T = 0; T < 3; ++T) *(LAS f32x4*)(xw + (t * 3 + T) * 1024) = kp ? acc[t][T] : acc[t][3 + T];
    }
    LGBAR();
    {
        lptr xr = sm + (wid ^ 2) * 6144 + lane * 16;
        const int ks = (e0 >> 5) + wm;
        const int nb = n0 / 96;
#pragma unroll
        for (int T = 0; T < 3; ++T) {
            f32x4 o0 = *(LAS f32x4*)(xr + T * 1024), o1 = *(LAS f32x4*)(xr + (3 + T) * 1024);
            f32x4 m0 = kp ? acc[0][3 + T] : acc[0][T], m1 = kp ? acc[1][3 + T] : acc[1][T];
            m0 += o0;
            m1 += o1;
            u32x4 o = {cvtpk(m0[0], m0[1]), cvtpk(m0[2], m0[3]), cvtpk(m1[0], m1[1]), cvtpk(m1[2], m1[3])};
            int Tp = 3 * kp + T;
            int Tf = 2 * (Tp >> 1) + ((r16 >> 2) & 1);
            int Rf = 4 * (2 * (Tp & 1) + (r16 >> 3)) + (r16 & 3);
            *(u32x4*)(w2f + ((size_t)((nb * 20 + ks) * 6 + Tf)) * 1024 + ((g << 4) | Rf) * 16) = o;
        }
    }
    STAMP(0, 10);
}

DEVI void xr_loader(unsigned smb, int lw, int m0, int e0, const unsigned short* __restrict__ x, const unsigned short* __restrict__ U) {
    const int lane = threadIdx.x & 63;
    unsigned vx[4], vu[5];
#pragma unroll
    for (int u = 0; u < 4; ++u) {
        int row = 8 * u + (lane >> 3);
        int c = (lane & 7) ^ ((row >> 1) & 7);
        vx[u] = (unsigned)((m0 + 32 * lw + row) * (D_ * 2) + c * 16 - u * 1024);
    }
#pragma unroll
    for (int u = 0; u < 5; ++u) {
        int q = (5 * lw + u) * 64 + lane;
        int k = q / 20, c = q % 20;
        c ^= 2 * ((k >> 3) & 1);
        vu[u] = (unsigned)(k * (D_ * 2) + e0 * 2 + c * 16 - (u & 3) * 1024);
    }
    const char* px = (const char*)x;
    const char* pu = (const char*)U;
    unsigned la = smb + lw * 4096;
    const unsigned luo = 16384 - lw * 4096 + lw * 5120;
    const unsigned lend = la + B_RING * B_SLOT;
    auto issue = [&]() {
        glds4(vx[0], vx[1], vx[2], vx[3], px, la);
        glds4(vu[0], vu[1], vu[2], vu[3], pu, la + luo);
        glds1(vu[4], pu, la + luo + 4096);
        px += 128;
        pu += 64 * D_ * 2;
        la += B_SLOT;
        if (la == lend) la -= B_RING * B_SLOT;
    };
    issue(); issue();
    VMBAR(9);
    issue(); issue();
#pragma unroll 1
    for (int s = 0; s < 6; ++s) {
        VMBAR(18);
        issue();
    }
    VMBAR(18);
    VMBAR(9);
    VMBAR(0);
}

struct BRegs {
    u32x4 x[2];
    u32x4 u[10];
};

DEVI void xr_compute(lptr sm, int wid, int m0, int e0, char* __restrict__ xrf STAMPARG) {
    const int tid = threadIdx.x, lane = tid & 63;
    const int g = lane >> 4, r16 = lane & 15;
    STAMP(0, 0);
    const int sw = (r16 >> 1) & 7;
    const int ox0 = wid * 4096 + r16 * 128 + ((g ^ sw) << 4);
    const int ox1 = wid * 4096 + r16 * 128 + (((4 + g) ^ sw) << 4);
    const int q4 = r16 >> 2, pp = r16 & 3;
    const int ourow = 16384 + (8 * g + q4) * 320 + 8 * pp;
    const int ouE = ourow + 32 * (g & 1), ouO = ourow - 32 * (g & 1);

    f32x4 acc[2][10];
#pragma unroll
    for (int t = 0; t < 2; ++t)
#pragma unroll
        for (int T = 0; T < 10; ++T) acc[t][T] = f32x4{0.f, 0.f, 0.f, 0.f};

    BRegs R0, R1;
    auto uload = [&](BRegs& r, lptr puE, lptr puO, int T) {
        lptr p = ((T & 1) ? puO : puE) + 32 * T;
        u32x2 lo = trread(p), hi = trread(p + 4 * 320);
        r.u[T] = u32x4{lo[0], lo[1], hi[0], hi[1]};
    };
    auto step = [&](BRegs& cur, BRegs& nxt, lptr px, lptr puE, lptr puO) {
        SB();
        nxt.x[0] = *(LAS u32x4*)(px);
        nxt.x[1] = *(LAS u32x4*)(px + 2048);
#pragma unroll
        for (int T = 0; T < 10; ++T) {
            acc[0][T] = mfma(cur.u[T], cur.x[0], acc[0][T]);
            uload(nxt, puE, puO, T);
            acc[1][T] = mfma(cur.u[T], cur.x[1], acc[1][T]);
            SB();
        }
    };
    STAMP(0, 1);
    LGBAR();
    STAMP(0, 2);
    R0.x[0] = *(LAS u32x4*)(sm + ox0);
    R0.x[1] = *(LAS u32x4*)(sm + ox0 + 2048);
#pragma unroll
    for (int T = 0; T < 10; ++T) uload(R0, sm + ouE, sm + ouO, T);
    int slotoff = 0;
#pragma unroll 1
    for (int s = 0; s < 9; ++s) {
        if (s % 2 == 0) STAMP(0, 3 + s / 2);
        lptr b = sm + slotoff;
        step(R0, R1, b + ox1, b + ouE + 32 * 320, b + ouO + 32 * 320);
        slotoff = (slotoff == (B_RING - 1) * B_SLOT) ? 0 : slotoff + B_SLOT;
        b = sm + slotoff;
        LGBAR();
        step(R1, R0, b + ox0, b + ouE, b + ouO);
    }
    {
        lptr b = sm + slotoff;
        step(R0, R1, b + ox1, b + ouE + 32 * 320, b + ouO + 32 * 320);
#pragma unroll
        for (int T = 0; T < 10; ++T) {
            acc[0][T] = mfma(R1.u[T], R1.x[0], acc[0][T]);
            acc[1][T] = mfma(R1.u[T], R1.x[1], acc[1][T]);
        }
    }
    STAMP(0, 9);
    {
        const int mb = (m0 >> 5) + wid;
        const int ks0 = e0 >> 5;
#pragma unroll
        for (int kq = 0; kq < 5; ++kq)
#pragma unroll
            for (int t = 0; t < 2; ++t) {
                f32x4 a0 = acc[t][2 * kq], a1 = acc[t][2 * kq + 1];
                u32x4 o = {cvtpk(a0[0], a0[1]), cvtpk(a0[2], a0[3]), cvtpk(a1[0], a1[1]), cvtpk(a1[2], a1[3])};
                *(u32x4*)(xrf + ((size_t)((mb * 20 + ks0 + kq) * 2 + t)) * 1024 + lane * 16) = o;
            }
    }
    STAMP(0, 10);
}

__global__ __launch_bounds__(512, 2) void k1(const unsigned short* __restrict__ x, const int* __restrict__ qw,
                                             const unsigned short* __restrict__ sc, const unsigned short* __restrict__ zr,
                                             const unsigned short* __restrict__ U, const unsigned short* __restrict__ V,
                                             char* __restrict__ xrf, char* __restrict__ w2f STAMPARG) {
    __shared__ __attribute__((aligned(1024))) char smem[K1_LDS];
    lptr sm = (lptr)smem;
    unsigned smb = (unsigned)(uintptr_t)sm;
    const int xcd = blockIdx.x & 7, idx = blockIdx.x >> 3;
    const int wid = __builtin_amdgcn_readfirstlane(threadIdx.x >> 6);
    const int xi = xcd >> 1, xj = xcd & 1;
    if (idx < 20) {
        const int ta = idx % 5, tb = idx / 5;
        const int e0 = 320 * (xi & 1) + 64 * ta;
        const int n0 = 768 * xj + 384 * (xi >> 1) + 96 * tb;
        if (wid >= 4) w2_loader(smb, wid - 4, e0, n0, qw, V);
        else w2_compute(sm, wid, e0, n0, sc, zr, w2f STAMPPASS);
    } else {
        const int i2 = idx - 20;
        const int m0 = 512 * xi + 128 * (i2 & 3);
        const int e0 = 320 * xj + 160 * (i2 >> 2);
        if (wid >= 4) xr_loader(smb, wid - 4, m0, e0, x, U);
        else xr_compute(sm, wid, m0, e0, xrf STAMPPASS);
    }
}

constexpr int C_SLOT = 28 * 1024;
constexpr int C_RING = 5;
struct CRegs {
    u32x4 x[2];
    u32x4 w[6];
};
__global__ __launch_bounds__(512, 2) void k2(const char* __restrict__ xrf, const char* __restrict__ w2f, unsigned short* __restrict__ y STAMPARG) {
    __shared__ __attribute__((aligned(1024))) char smem[C_SLOT * C_RING];
    lptr sm = (lptr)smem;
    unsigned smb = (unsigned)(uintptr_t)sm;
    const int tid = threadIdx.x, lane = tid & 63;
    const int wid = __builtin_amdgcn_readfirstlane(tid >> 6);
    const int xcd = blockIdx.x & 7, idx = blockIdx.x >> 3;
    const int xi = xcd >> 1, xj = xcd & 1;
    const int m0 = 512 * xi + 128 * (idx & 3);
    const int nb = 8 * xj + (idx >> 2);
    if (wid >= 4) {
        const int lw = wid - 4;
        const char* pw = w2f + (size_t)nb * (120 * 1024) + lw * 3072;
        const char* px = xrf + (size_t)((m0 >> 5) + lw) * (40 * 1024);
        const unsigned vl = lane * 16;
        unsigned la = smb + lw * 4096;
        const unsigned lwo = 16384 - lw * 4096 + lw * 3072;
        auto issue = [&]() {
            glds4(vl, vl, vl, vl, px, la);
            glds3(vl, vl, vl, pw, la + lwo);
            px += 4096;
            pw += 12288;
            la += C_SLOT;
        };
        issue(); issue(); issue();
        VMBAR(14);
        issue(); issue();
        la -= C_RING * C_SLOT;
#pragma unroll 1
        for (int s = 0; s < 5; ++s) {
            VMBAR(21);
            issue();
        }
        VMBAR(21);
        VMBAR(14);
        VMBAR(7);
        VMBAR(0);
        return;
    }
    STAMP(1, 0);
    const int g = lane >> 4, r16 = lane & 15;
    f32x4 acc[2][6];
#pragma unroll
    for (int t = 0; t < 2; ++t)
#pragma unroll
        for (int T = 0; T < 6; ++T) acc[t][T] = f32x4{0.f, 0.f, 0.f, 0.f};
    const int ox = wid * 4096 + lane * 16, ow = 16384 + lane * 16;
    CRegs R0, R1;
    auto step = [&](CRegs& cur, CRegs& nxt, lptr px, lptr pw) {
        SB();
        nxt.x[0] = *(LAS u32x4*)(px);
        nxt.x[1] = *(LAS u32x4*)(px + 1024);
#pragma unroll
        for (int T = 0; T < 6; ++T) {
            acc[0][T] = mfma(cur.w[T], cur.x[0], acc[0][T]);
            nxt.w[T] = *(LAS u32x4*)(pw + T * 1024);
            acc[1][T] = mfma(cur.w[T], cur.x[1], acc[1][T]);
            SB();
        }
    };
    STAMP(1, 1);
    LGBAR();
    STAMP(1, 2);
    R0.x[0] = *(LAS u32x4*)(sm + ox);
    R0.x[1] = *(LAS u32x4*)(sm + ox + 1024);
#pragma unroll
    for (int T = 0; T < 6; ++T) R0.w[T] = *(LAS u32x4*)(sm + ow + T * 1024);
    int slotoff = 0;
#pragma unroll 1
    for (int s = 0; s < 9; ++s) {
        if (s % 2 == 0) STAMP(1, 3 + s / 2);
        lptr b = sm + slotoff;
        step(R0, R1, b + ox + 2048, b + ow + 6144);
        slotoff = (slotoff == (C_RING - 1) * C_SLOT) ? 0 : slotoff + C_SLOT;
        b = sm + slotoff;
        LGBAR();
        step(R1, R0, b + ox, b + ow);
    }
    {
        lptr b = sm + slotoff;
        step(R0, R1, b + ox + 2048, b + ow + 6144);
#pragma unroll
        for (int T = 0; T < 6; ++T) {
            acc[0][T] = mfma(R1.w[T], R1.x[0], acc[0][T]);
            acc[1][T] = mfma(R1.w[T], R1.x[1], acc[1][T]);
        }
    }
    STAMP(1, 9);
#pragma unroll
    for (int t = 0; t < 2; ++t) {
        unsigned short* yr = y + (size_t)(m0 + 32 * wid + 16 * t + r16) * F_ + nb * 96 + 8 * g;
#pragma unroll
        for (int p = 0; p < 3; ++p) {
            f32x4 a0 = acc[t][2 * p], a1 = acc[t][2 * p + 1];
            u32x4 o = {cvtpk(a0[0], a0[1]), cvtpk(a0[2], a0[3]), cvtpk(a1[0], a1[1]), cvtpk(a1[2], a1[3])};
            *(u32x4*)(yr + 32 * p) = o;
        }
    }
    STAMP(1, 10);
}

__global__ __launch_bounds__(256) void k3(const unsigned long long* __restrict__ stamps, int nb1) {
    __shared__ unsigned long long s[8][256];
    const int t = threadIdx.x;
    const bool v1 = t < nb1;
    const int t1 = v1 ? t : 0;
    s[0][t] = stamps[(0 * 256 + t1) * 16 + 0];
    s[1][t] = stamps[(0 * 256 + t1) * 16 + 10];
    s[2][t] = stamps[(1 * 256 + t) * 16 + 0];
    s[3][t] = stamps[(1 * 256 + t) * 16 + 10];
    s[4][t] = stamps[(0 * 256 + t1) * 16 + 2];
    s[5][t] = stamps[(2 * 256 + t1) * 16 + 10] - stamps[(2 * 256 + t1) * 16 + 0];
    s[6][t] = stamps[(1 * 256 + t) * 16 + 2];
    s[7][t] = stamps[(0 * 256 + t1) * 16 + 9];
    __syncthreads();
    if (t == 0) {
        unsigned long long t0 = __builtin_amdgcn_s_memrealtime();
        unsigned long long mn0 = ~0ull, mx0 = 0, mx1 = 0, mn2 = ~0ull, mx3 = 0, sum4 = 0, sum6 = 0, sumA = 0, sumB = 0, mxA = 0, mxB = 0, sumL = 0;
        for (int i = 0; i < 256; ++i) {
            mn0 = s[0][i] < mn0 ? s[0][i] : mn0;
            mx0 = s[0][i] > mx0 ? s[0][i] : mx0;
            mx1 = s[1][i] > mx1 ? s[1][i] : mx1;
            mn2 = s[2][i] < mn2 ? s[2][i] : mn2;
            mx3 = s[3][i] > mx3 ? s[3][i] : mx3;
            if (i < 160) { sum4 += s[4][i] - s[0][i]; sumA += s[1][i] - s[0][i]; sumL += s[7][i] - s[4][i]; mxA = s[1][i] > mxA ? s[1][i] : mxA; }
            else if (i < 224) { sumB += s[1][i] - s[0][i]; mxB = s[1][i] > mxB ? s[1][i] : mxB; }
            sum6 += s[6][i] - s[2][i];
        }
        unsigned long long q[16];
        q[0] = 0;
        q[1] = (mx1 - mn0) * 10;
        q[2] = (mn2 - mx1) * 10;
        q[3] = (mx3 - mn2) * 10;
        q[4] = sum4 * 10 / 160;
        q[5] = s[5][0] * 100 / (s[1][0] - s[0][0]);
        q[6] = sum6 * 10 / 256;
        q[7] = (mx0 - mn0) * 10;
        q[8] = sumA * 10 / 160;
        q[9] = sumB * 10 / 64;
        q[10] = (mxB - mn0) * 10;
        q[11] = sumL * 10 / 160;
        q[12] = (t0 - mx3) * 10;
        unsigned long long w = q[PROBE];
        if (w > 60000) w = 60000;
        while (__builtin_amdgcn_s_memrealtime() - t0 < w) __builtin_amdgcn_s_sleep(1);
    }
}
extern "C" void kernel_launch(void* const* d_in, const int* in_sizes, int n_in, void* d_out, int out_size, void* d_ws,
                              size_t ws_size, hipStream_t stream) {
    const unsigned short* x = (const unsigned short*)d_in[0];
    const int* qw = (const int*)d_in[1];
    const unsigned short* sc = (const unsigned short*)d_in[2];
    const unsigned short* zr = (const unsigned short*)d_in[3];
    const unsigned short* U = (const unsigned short*)d_in[4];
    const unsigned short* V = (const unsigned short*)d_in[5];
    char* xrf = (char*)d_ws;
    char* w2f = xrf + (size_t)S_ * D_ * 2;
    unsigned long long* stamps = (unsigned long long*)((char*)d_ws + ws_size - (1 << 20));
    k1<<<224, 512, 0, stream>>>(x, qw, sc, zr, U, V, xrf, w2f STAMPPASS);
    k2<<<256, 512, 0, stream>>>(xrf, w2f, (unsigned short*)d_out STAMPPASS);
    k3<<<1, 256, 0, stream>>>(stamps, 224);
}
```

```cpp
#define STAMPS
#define PROBE 14
#include <hip/hip_runtime.h>
#include <stdint.h>

typedef __attribute__((ext_vector_type(8))) __bf16 bf16x8;
typedef __attribute__((ext_vector_type(2))) __bf16 bf16x2;
typedef __attribute__((ext_vector_type(4))) float f32x4;
typedef __attribute__((ext_vector_type(2))) float f32x2;
typedef __attribute__((ext_vector_type(4))) unsigned u32x4;
typedef __attribute__((ext_vector_type(4))) int i32x4;
typedef __attribute__((ext_vector_type(2))) unsigned u32x2;
typedef short v4i16 __attribute__((ext_vector_type(4)));
#define LAS __attribute__((address_space(3)))
typedef LAS char* lptr;

#define DEVI __device__ __forceinline__
#define SB() __builtin_amdgcn_sched_barrier(0)

constexpr int S_ = 2048, D_ = 640, F_ = 1536;

DEVI void glds4(unsigned v0, unsigned v1, unsigned v2, unsigned v3, const void* sbase, unsigned lds) {
    asm volatile("s_mov_b32 m0, %5\n\ts_nop 0\n\t"
                 "global_load_lds_dwordx4 %0, %4\n\t"
                 "global_load_lds_dwordx4 %1, %4 offset:1024\n\t"
                 "global_load_lds_dwordx4 %2, %4 offset:2048\n\t"
                 "global_load_lds_dwordx4 %3, %4 offset:3072"
                 ::"v"(v0), "v"(v1), "v"(v2), "v"(v3), "s"(sbase), "s"(lds) : "memory");
}
DEVI void glds3(unsigned v0, unsigned v1, unsigned v2, const void* sbase, unsigned lds) {
    asm volatile("s_mov_b32 m0, %4\n\ts_nop 0\n\t"
                 "global_load_lds_dwordx4 %0, %3\n\t"
                 "global_load_lds_dwordx4 %1, %3 offset:1024\n\t"
                 "global_load_lds_dwordx4 %2, %3 offset:2048"
                 ::"v"(v0), "v"(v1), "v"(v2), "s"(sbase), "s"(lds) : "memory");
}
DEVI void glds1(unsigned v0, const void* sbase, unsigned lds) {
    asm volatile("s_mov_b32 m0, %2\n\ts_nop 0\n\t"
                 "global_load_lds_dwordx4 %0, %1"
                 ::"v"(v0), "s"(sbase), "s"(lds) : "memory");
}
DEVI unsigned cvtpk(float lo, float hi) {
    f32x2 v = {lo, hi};
    return __builtin_bit_cast(unsigned, __builtin_convertvector(v, bf16x2));
}
DEVI float fma1(float a, float b, float c) {
    float r;
    asm("v_fma_f32 %0, %1, %2, %3" : "=v"(r) : "v"(a), "v"(b), "v"(c));
    return r;
}
DEVI u32x2 trread(lptr p) {
    return __builtin_bit_cast(u32x2, __builtin_amdgcn_ds_read_tr16_b64_v4i16((LAS v4i16*)p));
}
DEVI f32x4 mfma(u32x4 a, u32x4 b, f32x4 c) {
    return __builtin_amdgcn_mfma_f32_16x16x32_bf16(__builtin_bit_cast(bf16x8, a), __builtin_bit_cast(bf16x8, b), c, 0, 0, 0);
}
#define STAMP(k, i) do { if (threadIdx.x == 0) { unsigned long long t_ = __builtin_amdgcn_s_memrealtime(); stamps[((k) * 256 + blockIdx.x) * 16 + (i)] = t_; stamps[((2 + k) * 256 + blockIdx.x) * 16 + (i)] = __builtin_amdgcn_s_memtime(); } } while (0)
#define STAMPARG , unsigned long long* __restrict__ stamps
#define STAMPPASS , stamps
#define VMBAR(N) asm volatile("s_waitcnt vmcnt(" #N ")\n\ts_barrier" ::: "memory")
#define LGBAR() asm volatile("s_waitcnt lgkmcnt(0)\n\ts_barrier" ::: "memory")

constexpr int A_SLOT = 28 * 1024;
constexpr int A_RING = 5;
constexpr int A_TAB = A_SLOT * A_RING;
constexpr int K1_LDS = A_TAB + 12 * 1024;
constexpr int B_SLOT = 36 * 1024;
constexpr int B_RING = 4;

struct ARegs {
    u32x4 a[2];
    u32x4 v[6];
};

DEVI u32x4 deq8(i32x4 q0, i32x4 q1, f32x4 s0, f32x4 z0, f32x4 s1, f32x4 z1) {
    float f[8];
#pragma unroll
    for (int j = 0; j < 4; ++j) {
        f[j] = fma1((float)q0[j], s0[j], z0[j]);
        f[4 + j] = fma1((float)q1[j], s1[j], z1[j]);
    }
    return u32x4{cvtpk(f[0], f[1]), cvtpk(f[2], f[3]), cvtpk(f[4], f[5]), cvtpk(f[6], f[7])};
}
DEVI u32x2 deq4(i32x4 q, f32x4 s, f32x4 z) {
    float f[4];
#pragma unroll
    for (int j = 0; j < 4; ++j) f[j] = fma1((float)q[j], s[j], z[j]);
    return u32x2{cvtpk(f[0], f[1]), cvtpk(f[2], f[3])};
}

DEVI void w2_loader(unsigned smb, int lw, int e0, int n0, const int* __restrict__ qw, const unsigned short* __restrict__ V) {
    const int lane = threadIdx.x & 63;
    const int wm = lw & 1, kp = lw >> 1;
    unsigned va[4], vv[3];
#pragma unroll
    for (int u = 0; u < 4; ++u) {
        int row = 8 * u + (lane >> 3);
        int c = (lane & 7) ^ ((row >> 1) & 7);
        va[u] = (unsigned)((e0 + 32 * wm + row) * (F_ * 4) + kp * 128 + c * 16 - u * 1024);
    }
#pragma unroll
    for (int u = 0; u < 3; ++u) {
        int q = (3 * lw + u) * 64 + lane;
        int k = q / 12, c = q % 12;
        c ^= 2 * ((k >> 2) & 1);
        vv[u] = (unsigned)(k * (F_ * 2) + n0 * 2 + c * 16 - u * 1024);
    }
    const char* pa = (const char*)qw;
    const char* pv = (const char*)V;
    unsigned la = smb + lw * 4096;
    const unsigned lvo = 16384 - lw * 4096 + lw * 3072;
    const unsigned lend = la + A_RING * A_SLOT;
    auto issue = [&]() {
        glds4(va[0], va[1], va[2], va[3], pa, la);
        glds3(vv[0], vv[1], vv[2], pv, la + lvo);
        pa += 256;
        pv += 64 * F_ * 2;
        la += A_SLOT;
        if (la == lend) la -= A_RING * A_SLOT;
    };
    issue(); issue(); issue();
    VMBAR(14);
    issue(); issue();
#pragma unroll 1
    for (int s = 0; s < 19; ++s) {
        VMBAR(21);
        issue();
    }
    VMBAR(21);
    VMBAR(14);
    VMBAR(7);
    VMBAR(0);
}

DEVI void w2_compute(lptr sm, int wid, int e0, int n0, const unsigned short* __restrict__ sc, const unsigned short* __restrict__ zr,
                     char* __restrict__ w2f STAMPARG) {
    const int tid = threadIdx.x, lane = tid & 63;
    const int wm = wid & 1, kp = wid >> 1;
    const int g = lane >> 4, r16 = lane & 15;
    STAMP(0, 0);
    if (tid < 192) {
        u32x4 tsc = *(const u32x4*)(sc + tid * 8), tzr = *(const u32x4*)(zr + tid * 8);
        lptr tp = sm + A_TAB + tid * 64;
#pragma unroll
        for (int h = 0; h < 2; ++h) {
            f32x4 s, z;
            s[0] = __uint_as_float(tsc[2 * h] << 16);
            s[1] = __uint_as_float(tsc[2 * h] & 0xffff0000u);
            s[2] = __uint_as_float(tsc[2 * h + 1] << 16);
            s[3] = __uint_as_float(tsc[2 * h + 1] & 0xffff0000u);
            z[0] = -__uint_as_float(tzr[2 * h] << 16) * s[0];
            z[1] = -__uint_as_float(tzr[2 * h] & 0xffff0000u) * s[1];
            z[2] = -__uint_as_float(tzr[2 * h + 1] << 16) * s[2];
            z[3] = -__uint_as_float(tzr[2 * h + 1] & 0xffff0000u) * s[3];
            *(LAS f32x4*)(tp + h * 32) = s;
            *(LAS f32x4*)(tp + h * 32 + 16) = z;
        }
    }
    const int sw = (r16 >> 1) & 7;
    const int oa0 = wid * 4096 + r16 * 128 + ((g ^ sw) << 4);
    const int oa1 = wid * 4096 + r16 * 128 + (((4 + g) ^ sw) << 4);
    const int q4 = r16 >> 2, pp = r16 & 3;
    const int ovrow = 16384 + (32 * kp + 4 * g + q4) * 192 + 8 * pp;
    const int ovE = ovrow + 32 * (g & 1), ovO = ovrow - 32 * (g & 1);
    lptr ptab = sm + A_TAB + (8 * kp + g) * 32;

    f32x4 acc[2][6];
#pragma unroll
    for (int t = 0; t < 2; ++t)
#pragma unroll
        for (int T = 0; T < 6; ++T) acc[t][T] = f32x4{0.f, 0.f, 0.f, 0.f};

    ARegs R0, R1;
    auto vload = [&](ARegs& r, lptr pvE, lptr pvO, int T) {
        lptr p = ((T & 1) ? pvO : pvE) + 32 * T;
        u32x2 lo = trread(p), hi = trread(p + 16 * 192);
        r.v[T] = u32x4{lo[0], lo[1], hi[0], hi[1]};
    };
    STAMP(0, 1);
    LGBAR();
    STAMP(0, 2);
    {
        i32x4 q00 = *(LAS i32x4*)(sm + oa0), q01 = *(LAS i32x4*)(sm + oa1);
        i32x4 q10 = *(LAS i32x4*)(sm + oa0 + 2048), q11 = *(LAS i32x4*)(sm + oa1 + 2048);
        f32x4 s0 = *(LAS f32x4*)(ptab), z0 = *(LAS f32x4*)(ptab + 16);
        f32x4 s1 = *(LAS f32x4*)(ptab + 128), z1 = *(LAS f32x4*)(ptab + 144);
#pragma unroll
        for (int T = 0; T < 6; ++T) vload(R0, sm + ovE, sm + ovO, T);
        R0.a[0] = deq8(q00, q01, s0, z0, s1, z1);
        R0.a[1] = deq8(q10, q11, s0, z0, s1, z1);
    }
    int slotoff = 0;
    auto body = [&](ARegs& cur, ARegs& nxt) {
        slotoff = (slotoff == (A_RING - 1) * A_SLOT) ? 0 : slotoff + A_SLOT;
        ptab += 512;
        lptr b = sm + slotoff;
        lptr pa0 = b + oa0, pa1 = b + oa1, pvE = b + ovE, pvO = b + ovO;
        LGBAR();
        SB();
        i32x4 q00 = *(LAS i32x4*)(pa0), q01 = *(LAS i32x4*)(pa1);
        f32x4 s0 = *(LAS f32x4*)(ptab), z0 = *(LAS f32x4*)(ptab + 16);
        acc[0][0] = mfma(cur.a[0], cur.v[0], acc[0][0]);
        f32x4 s1 = *(LAS f32x4*)(ptab + 128), z1 = *(LAS f32x4*)(ptab + 144);
        acc[1][0] = mfma(cur.a[1], cur.v[0], acc[1][0]);
        i32x4 q10 = *(LAS i32x4*)(pa0 + 2048), q11 = *(LAS i32x4*)(pa1 + 2048);
        SB();
        acc[0][1] = mfma(cur.a[0], cur.v[1], acc[0][1]);
        vload(nxt, pvE, pvO, 0);
        SB();
        acc[1][1] = mfma(cur.a[1], cur.v[1], acc[1][1]);
        vload(nxt, pvE, pvO, 1);
        SB();
        acc[0][2] = mfma(cur.a[0], cur.v[2], acc[0][2]);
        vload(nxt, pvE, pvO, 2);
        SB();
        acc[1][2] = mfma(cur.a[1], cur.v[2], acc[1][2]);
        vload(nxt, pvE, pvO, 3);
        SB();
        acc[0][3] = mfma(cur.a[0], cur.v[3], acc[0][3]);
        u32x2 a00 = deq4(q00, s0, z0);
        SB();
        acc[1][3] = mfma(cur.a[1], cur.v[3], acc[1][3]);
        vload(nxt, pvE, pvO, 4);
        u32x2 a01 = deq4(q01, s1, z1);
        SB();
        acc[0][4] = mfma(cur.a[0], cur.v[4], acc[0][4]);
        vload(nxt, pvE, pvO, 5);
        u32x2 a10 = deq4(q10, s0, z0);
        SB();
        acc[1][4] = mfma(cur.a[1], cur.v[4], acc[1][4]);
        u32x2 a11 = deq4(q11, s1, z1);
        SB();
        acc[0][5] = mfma(cur.a[0], cur.v[5], acc[0][5]);
        acc[1][5] = mfma(cur.a[1], cur.v[5], acc[1][5]);
        nxt.a[0] = u32x4{a00[0], a00[1], a01[0], a01[1]};
        nxt.a[1] = u32x4{a10[0], a10[1], a11[0], a11[1]};
        SB();
    };
#pragma unroll 1
    for (int s = 0; s < 22; s += 2) {
        if (s % 4 == 0) STAMP(0, 3 + s / 4);
        body(R0, R1);
        body(R1, R0);
    }
    body(R0, R1);
#pragma unroll
    for (int T = 0; T < 6; ++T) {
        acc[0][T] = mfma(R1.a[0], R1.v[T], acc[0][T]);
        acc[1][T] = mfma(R1.a[1], R1.v[T], acc[1][T]);
    }
    STAMP(0, 9);
    {
        lptr xw = sm + wid * 6144 + lane * 16;
#pragma unroll
        for (int t = 0; t < 2; ++t)
#pragma unroll
            for (int T = 0; T < 3; ++T) *(LAS f32x4*)(xw + (t * 3 + T) * 1024) = kp ? acc[t][T] : acc[t][3 + T];
    }
    LGBAR();
    {
        lptr xr = sm + (wid ^ 2) * 6144 + lane * 16;
        const int ks = (e0 >> 5) + wm;
        const int nb = n0 / 96;
#pragma unroll
        for (int T = 0; T < 3; ++T) {
            f32x4 o0 = *(LAS f32x4*)(xr + T * 1024), o1 = *(LAS f32x4*)(xr + (3 + T) * 1024);
            f32x4 m0 = kp ? acc[0][3 + T] : acc[0][T], m1 = kp ? acc[1][3 + T] : acc[1][T];
            m0 += o0;
            m1 += o1;
            u32x4 o = {cvtpk(m0[0], m0[1]), cvtpk(m0[2], m0[3]), cvtpk(m1[0], m1[1]), cvtpk(m1[2], m1[3])};
            int Tp = 3 * kp + T;
            int Tf = 2 * (Tp >> 1) + ((r16 >> 2) & 1);
            int Rf = 4 * (2 * (Tp & 1) + (r16 >> 3)) + (r16 & 3);
            *(u32x4*)(w2f + ((size_t)((nb * 20 + ks) * 6 + Tf)) * 1024 + ((g << 4) | Rf) * 16) = o;
        }
    }
    STAMP(0, 10);
}

DEVI void xr_loader(unsigned smb, int lw, int m0, int e0, const unsigned short* __restrict__ x, const unsigned short* __restrict__ U) {
    const int lane = threadIdx.x & 63;
    unsigned vx[4], vu[5];
#pragma unroll
    for (int u = 0; u < 4; ++u) {
        int row = 8 * u + (lane >> 3);
        int c = (lane & 7) ^ ((row >> 1) & 7);
        vx[u] = (unsigned)((m0 + 32 * lw + row) * (D_ * 2) + c * 16 - u * 1024);
    }
#pragma unroll
    for (int u = 0; u < 5; ++u) {
        int q = (5 * lw + u) * 64 + lane;
        int k = q / 20, c = q % 20;
        c ^= 2 * ((k >> 3) & 1);
        vu[u] = (unsigned)(k * (D_ * 2) + e0 * 2 + c * 16 - (u & 3) * 1024);
    }
    const char* px = (const char*)x;
    const char* pu = (const char*)U;
    unsigned la = smb + lw * 4096;
    const unsigned luo = 16384 - lw * 4096 + lw * 5120;
    const unsigned lend = la + B_RING * B_SLOT;
    auto issue = [&]() {
        glds4(vx[0], vx[1], vx[2], vx[3], px, la);
        glds4(vu[0], vu[1], vu[2], vu[3], pu, la + luo);
        glds1(vu[4], pu, la + luo + 4096);
        px += 128;
        pu += 64 * D_ * 2;
        la += B_SLOT;
        if (la == lend) la -= B_RING * B_SLOT;
    };
    issue(); issue();
    VMBAR(9);
    issue(); issue();
#pragma unroll 1
    for (int s = 0; s < 6; ++s) {
        VMBAR(18);
        issue();
    }
    VMBAR(18);
    VMBAR(9);
    VMBAR(0);
}

struct BRegs {
    u32x4 x[2];
    u32x4 u[10];
};

DEVI void xr_compute(lptr sm, int wid, int m0, int e0, char* __restrict__ xrf STAMPARG) {
    const int tid = threadIdx.x, lane = tid & 63;
    const int g = lane >> 4, r16 = lane & 15;
    STAMP(0, 0);
    const int sw = (r16 >> 1) & 7;
    const int ox0 = wid * 4096 + r16 * 128 + ((g ^ sw) << 4);
    const int ox1 = wid * 4096 + r16 * 128 + (((4 + g) ^ sw) << 4);
    const int q4 = r16 >> 2, pp = r16 & 3;
    const int ourow = 16384 + (8 * g + q4) * 320 + 8 * pp;
    const int ouE = ourow + 32 * (g & 1), ouO = ourow - 32 * (g & 1);

    f32x4 acc[2][10];
#pragma unroll
    for (int t = 0; t < 2; ++t)
#pragma unroll
        for (int T = 0; T < 10; ++T) acc[t][T] = f32x4{0.f, 0.f, 0.f, 0.f};

    BRegs R0, R1;
    auto uload = [&](BRegs& r, lptr puE, lptr puO, int T) {
        lptr p = ((T & 1) ? puO : puE) + 32 * T;
        u32x2 lo = trread(p), hi = trread(p + 4 * 320);
        r.u[T] = u32x4{lo[0], lo[1], hi[0], hi[1]};
    };
    auto step = [&](BRegs& cur, BRegs& nxt, lptr px, lptr puE, lptr puO) {
        SB();
        nxt.x[0] = *(LAS u32x4*)(px);
        nxt.x[1] = *(LAS u32x4*)(px + 2048);
#pragma unroll
        for (int T = 0; T < 10; ++T) {
            acc[0][T] = mfma(cur.u[T], cur.x[0], acc[0][T]);
            uload(nxt, puE, puO, T);
            acc[1][T] = mfma(cur.u[T], cur.x[1], acc[1][T]);
            SB();
        }
    };
    STAMP(0, 1);
    LGBAR();
    STAMP(0, 2);
    R0.x[0] = *(LAS u32x4*)(sm + ox0);
    R0.x[1] = *(LAS u32x4*)(sm + ox0 + 2048);
#pragma unroll
    for (int T = 0; T < 10; ++T) uload(R0, sm + ouE, sm + ouO, T);
    int slotoff = 0;
#pragma unroll 1
    for (int s = 0; s < 9; ++s) {
        if (s % 2 == 0) STAMP(0, 3 + s / 2);
        lptr b = sm + slotoff;
        step(R0, R1, b + ox1, b + ouE + 32 * 320, b + ouO + 32 * 320);
        slotoff = (slotoff == (B_RING - 1) * B_SLOT) ? 0 : slotoff + B_SLOT;
        b = sm + slotoff;
        LGBAR();
        step(R1, R0, b + ox0, b + ouE, b + ouO);
    }
    {
        lptr b = sm + slotoff;
        step(R0, R1, b + ox1, b + ouE + 32 * 320, b + ouO + 32 * 320);
#pragma unroll
        for (int T = 0; T < 10; ++T) {
            acc[0][T] = mfma(R1.u[T], R1.x[0], acc[0][T]);
            acc[1][T] = mfma(R1.u[T], R1.x[1], acc[1][T]);
        }
    }
    STAMP(0, 9);
    {
        const int mb = (m0 >> 5) + wid;
        const int ks0 = e0 >> 5;
#pragma unroll
        for (int kq = 0; kq < 5; ++kq)
#pragma unroll
            for (int t = 0; t < 2; ++t) {
                f32x4 a0 = acc[t][2 * kq], a1 = acc[t][2 * kq + 1];
                u32x4 o = {cvtpk(a0[0], a0[1]), cvtpk(a0[2], a0[3]), cvtpk(a1[0], a1[1]), cvtpk(a1[2], a1[3])};
                *(u32x4*)(xrf + ((size_t)((mb * 20 + ks0 + kq) * 2 + t)) * 1024 + lane * 16) = o;
            }
    }
    STAMP(0, 10);
}

__global__ __launch_bounds__(512, 2) void k1(const unsigned short* __restrict__ x, const int* __restrict__ qw,
                                             const unsigned short* __restrict__ sc, const unsigned short* __restrict__ zr,
                                             const unsigned short* __restrict__ U, const unsigned short* __restrict__ V,
                                             char* __restrict__ xrf, char* __restrict__ w2f STAMPARG) {
    __shared__ __attribute__((aligned(1024))) char smem[K1_LDS];
    lptr sm = (lptr)smem;
    unsigned smb = (unsigned)(uintptr_t)sm;
    const int xcd = blockIdx.x & 7, idx = blockIdx.x >> 3;
    const int wid = __builtin_amdgcn_readfirstlane(threadIdx.x >> 6);
    const int xi = xcd >> 1, xj = xcd & 1;
    if (idx < 20) {
        const int ta = idx % 5, tb = idx / 5;
        const int e0 = 320 * (xi & 1) + 64 * ta;
        const int n0 = 768 * xj + 384 * (xi >> 1) + 96 * tb;
        if (wid >= 4) w2_loader(smb, wid - 4, e0, n0, qw, V);
        else w2_compute(sm, wid, e0, n0, sc, zr, w2f STAMPPASS);
    } else {
        const int i2 = idx - 20;
        const int m0 = 512 * xi + 128 * (i2 & 3);
        const int e0 = 320 * xj + 160 * (i2 >> 2);
        if (wid >= 4) xr_loader(smb, wid - 4, m0, e0, x, U);
        else xr_compute(sm, wid, m0, e0, xrf STAMPPASS);
    }
}

constexpr int C_SLOT = 28 * 1024;
constexpr int C_RING = 5;
struct CRegs {
    u32x4 x[2];
    u32x4 w[6];
};
__global__ __launch_bounds__(512, 2) void k2(const char* __restrict__ xrf, const char* __restrict__ w2f, unsigned short* __restrict__ y STAMPARG) {
    __shared__ __attribute__((aligned(1024))) char smem[C_SLOT * C_RING];
    lptr sm = (lptr)smem;
    unsigned smb = (unsigned)(uintptr_t)sm;
    const int tid = threadIdx.x, lane = tid & 63;
    const int wid = __builtin_amdgcn_readfirstlane(tid >> 6);
    const int xcd = blockIdx.x & 7, idx = blockIdx.x >> 3;
    const int xi = xcd >> 1, xj = xcd & 1;
    const int m0 = 512 * xi + 128 * (idx & 3);
    const int nb = 8 * xj + (idx >> 2);
    if (wid >= 4) {
        const int lw = wid - 4;
        const char* pw = w2f + (size_t)nb * (120 * 1024) + lw * 3072;
        const char* px = xrf + (size_t)((m0 >> 5) + lw) * (40 * 1024);
        const unsigned vl = lane * 16;
        unsigned la = smb + lw * 4096;
        const unsigned lwo = 16384 - lw * 4096 + lw * 3072;
        auto issue = [&]() {
            glds4(vl, vl, vl, vl, px, la);
            glds3(vl, vl, vl, pw, la + lwo);
            px += 4096;
            pw += 12288;
            la += C_SLOT;
        };
        issue(); issue(); issue();
        VMBAR(14);
        issue(); issue();
        la -= C_RING * C_SLOT;
#pragma unroll 1
        for (int s = 0; s < 5; ++s) {
            VMBAR(21);
            issue();
        }
        VMBAR(21);
        VMBAR(14);
        VMBAR(7);
        VMBAR(0);
        return;
    }
    STAMP(1, 0);
    const int g = lane >> 4, r16 = lane & 15;
    f32x4 acc[2][6];
#pragma unroll
    for (int t = 0; t < 2; ++t)
#pragma unroll
        for (int T = 0; T < 6; ++T) acc[t][T] = f32x4{0.f, 0.f, 0.f, 0.f};
    const int ox = wid * 4096 + lane * 16, ow = 16384 + lane * 16;
    CRegs R0, R1;
    auto step = [&](CRegs& cur, CRegs& nxt, lptr px, lptr pw) {
        SB();
        nxt.x[0] = *(LAS u32x4*)(px);
        nxt.x[1] = *(LAS u32x4*)(px + 1024);
#pragma unroll
        for (int T = 0; T < 6; ++T) {
            acc[0][T] = mfma(cur.w[T], cur.x[0], acc[0][T]);
            nxt.w[T] = *(LAS u32x4*)(pw + T * 1024);
            acc[1][T] = mfma(cur.w[T], cur.x[1], acc[1][T]);
            SB();
        }
    };
    STAMP(1, 1);
    LGBAR();
    STAMP(1, 2);
    R0.x[0] = *(LAS u32x4*)(sm + ox);
    R0.x[1] = *(LAS u32x4*)(sm + ox + 1024);
#pragma unroll
    for (int T = 0; T < 6; ++T) R0.w[T] = *(LAS u32x4*)(sm + ow + T * 1024);
    int slotoff = 0;
#pragma unroll 1
    for (int s = 0; s < 9; ++s) {
        if (s % 2 == 0) STAMP(1, 3 + s / 2);
        lptr b = sm + slotoff;
        step(R0, R1, b + ox + 2048, b + ow + 6144);
        slotoff = (slotoff == (C_RING - 1) * C_SLOT) ? 0 : slotoff + C_SLOT;
        b = sm + slotoff;
        LGBAR();
        step(R1, R0, b + ox, b + ow);
    }
    {
        lptr b = sm + slotoff;
        step(R0, R1, b + ox + 2048, b + ow + 6144);
#pragma unroll
        for (int T = 0; T < 6; ++T) {
            acc[0][T] = mfma(R1.w[T], R1.x[0], acc[0][T]);
            acc[1][T] = mfma(R1.w[T], R1.x[1], acc[1][T]);
        }
    }
    STAMP(1, 9);
#pragma unroll
    for (int t = 0; t < 2; ++t) {
        unsigned short* yr = y + (size_t)(m0 + 32 * wid + 16 * t + r16) * F_ + nb * 96 + 8 * g;
#pragma unroll
        for (int p = 0; p < 3; ++p) {
            f32x4 a0 = acc[t][2 * p], a1 = acc[t][2 * p + 1];
            u32x4 o = {cvtpk(a0[0], a0[1]), cvtpk(a0[2], a0[3]), cvtpk(a1[0], a1[1]), cvtpk(a1[2], a1[3])};
            *(u32x4*)(yr + 32 * p) = o;
        }
    }
    STAMP(1, 10);
}

__global__ __launch_bounds__(256) void k3(const unsigned long long* __restrict__ stamps, int nb1) {
    __shared__ unsigned long long s[8][256];
    const int t = threadIdx.x;
    const bool v1 = t < nb1;
    const int t1 = v1 ? t : 0;
    s[0][t] = stamps[(0 * 256 + t1) * 16 + 0];
    s[1][t] = stamps[(0 * 256 + t1) * 16 + 10];
    s[2][t] = stamps[(1 * 256 + t) * 16 + 0];
    s[3][t] = stamps[(1 * 256 + t) * 16 + 10];
    s[4][t] = stamps[(0 * 256 + t1) * 16 + 2];
    s[5][t] = stamps[(2 * 256 + t1) * 16 + 10] - stamps[(2 * 256 + t1) * 16 + 0];
    s[6][t] = stamps[(1 * 256 + t) * 16 + 2];
    s[7][t] = stamps[(0 * 256 + t1) * 16 + 9];
    __syncthreads();
    if (t == 0) {
        unsigned long long t0 = __builtin_amdgcn_s_memrealtime();
        unsigned long long mn0 = ~0ull, mx0 = 0, mx1 = 0, mn2 = ~0ull, mx3 = 0, sum4 = 0, sum6 = 0, sumA = 0, sumB = 0, mxA = 0, mxB = 0, sumL = 0;
        for (int i = 0; i < 256; ++i) {
            mn0 = s[0][i] < mn0 ? s[0][i] : mn0;
            mx0 = s[0][i] > mx0 ? s[0][i] : mx0;
            mx1 = s[1][i] > mx1 ? s[1][i] : mx1;
            mn2 = s[2][i] < mn2 ? s[2][i] : mn2;
            mx3 = s[3][i] > mx3 ? s[3][i] : mx3;
            if (i < 160) { sum4 += s[4][i] - s[0][i]; sumA += s[1][i] - s[0][i]; sumL += s[7][i] - s[4][i]; mxA = s[1][i] > mxA ? s[1][i] : mxA; }
            else if (i < 224) { sumB += s[1][i] - s[0][i]; mxB = s[1][i] > mxB ? s[1][i] : mxB; }
            sum6 += s[6][i] - s[2][i];
        }
        unsigned long long q[16];
        q[0] = 0;
        q[1] = (mx1 - mn0) * 10;
        q[2] = (mn2 - mx1) * 10;
        q[3] = (mx3 - mn2) * 10;
        q[4] = sum4 * 10 / 160;
        q[5] = s[5][0] * 100 / (s[1][0] - s[0][0]);
        q[6] = sum6 * 10 / 256;
        q[7] = (mx0 - mn0) * 10;
        q[8] = sumA * 10 / 160;
        q[9] = sumB * 10 / 64;
        q[10] = (mxB - mn0) * 10;
        q[11] = sumL * 10 / 160;
        q[12] = (t0 - mx3) * 10;
        q[13] = (mn0 - stamps[(3 * 256 + 255) * 16 + 15]) * 10;
        q[14] = 0;
        unsigned long long w = q[PROBE];
        if (w > 60000) w = 60000;
        while (__builtin_amdgcn_s_memrealtime() - t0 < w) __builtin_amdgcn_s_sleep(1);
    }
}

__global__ __launch_bounds__(64) void k0(unsigned long long* __restrict__ stamps) {
    if (threadIdx.x == 0 && blockIdx.x == 0) stamps[(3 * 256 + 255) * 16 + 15] = __builtin_amdgcn_s_memrealtime();
}
extern "C" void kernel_launch(void* const* d_in, const int* in_sizes, int n_in, void* d_out, int out_size, void* d_ws,
                              size_t ws_size, hipStream_t stream) {
    const unsigned short* x = (const unsigned short*)d_in[0];
    const int* qw = (const int*)d_in[1];
    const unsigned short* sc = (const unsigned short*)d_in[2];
    const unsigned short* zr = (const unsigned short*)d_in[3];
    const unsigned short* U = (const unsigned short*)d_in[4];
    const unsigned short* V = (const unsigned short*)d_in[5];
    char* xrf = (char*)d_ws;
    char* w2f = xrf + (size_t)S_ * D_ * 2;
    unsigned long long* stamps = (unsigned long long*)((char*)d_ws + ws_size - (1 << 20));
    k0<<<256, 64, 0, stream>>>(stamps);
    k1<<<224, 512, 0, stream>>>(x, qw, sc, zr, U, V, xrf, w2f STAMPPASS);
    k2<<<256, 512, 0, stream>>>(xrf, w2f, (unsigned short*)d_out STAMPPASS);
    k3<<<1, 256, 0, stream>>>(stamps, 224);
}
```

```cpp
#include <hip/hip_runtime.h>
#include <stdint.h>

typedef __attribute__((ext_vector_type(8))) __bf16 bf16x8;
typedef __attribute__((ext_vector_type(2))) __bf16 bf16x2;
typedef __attribute__((ext_vector_type(4))) float f32x4;
typedef __attribute__((ext_vector_type(2))) float f32x2;
typedef __attribute__((ext_vector_type(4))) unsigned u32x4;
typedef __attribute__((ext_vector_type(4))) int i32x4;
typedef __attribute__((ext_vector_type(2))) unsigned u32x2;
typedef short v4i16 __attribute__((ext_vector_type(4)));
#define LAS __attribute__((address_space(3)))
typedef LAS char* lptr;

typedef unsigned long long u64x2 __attribute__((ext_vector_type(2)));
#define DEVI __device__ __forceinline__
#define SB() __builtin_amdgcn_sched_barrier(0)

constexpr int S_ = 2048, D_ = 640, F_ = 1536;

#define RX 11
#define P0 0
#define PA 6
#define PJ 12
#define TLW 0
#define TB 0
#define TD 0
#define XBAR 1
#define CPF 1
#define CP2 1
#define TOUCH_POL ""
#define DQ0 0
#define W_LA 11
#define X_LA 13
#define C_LA 11

#define K2T 0
#define K2TD 0
#define K2SK 0
#define YPOL "sc1"
#define WPOL ""
#define XPOL ""
#define HPOL ""
#define ST16(p, v, POL) asm volatile("global_store_dwordx4 %0, %1, off " POL "\n\ts_nop 1" ::"v"(p), "v"(v) : "memory")
#define ST8(p, v, POL) asm volatile("global_store_dwordx2 %0, %1, off " POL "\n\ts_nop 1" ::"v"(p), "v"(v) : "memory")

DEVI void glds4(unsigned v0, unsigned v1, unsigned v2, unsigned v3, const void* sbase, unsigned lds) {
    asm volatile("s_mov_b32 m0, %5\n\ts_nop 0\n\t"
                 "global_load_lds_dwordx4 %0, %4\n\t"
                 "global_load_lds_dwordx4 %1, %4 offset:1024\n\t"
                 "global_load_lds_dwordx4 %2, %4 offset:2048\n\t"
                 "global_load_lds_dwordx4 %3, %4 offset:3072"
                 ::"v"(v0), "v"(v1), "v"(v2), "v"(v3), "s"(sbase), "s"(lds) : "memory");
}
DEVI void glds3(unsigned v0, unsigned v1, unsigned v2, const void* sbase, unsigned lds) {
    asm volatile("s_mov_b32 m0, %4\n\ts_nop 0\n\t"
                 "global_load_lds_dwordx4 %0, %3\n\t"
                 "global_load_lds_dwordx4 %1, %3 offset:1024\n\t"
                 "global_load_lds_dwordx4 %2, %3 offset:2048"
                 ::"v"(v0), "v"(v1), "v"(v2), "s"(sbase), "s"(lds) : "memory");
}
DEVI void glds1(unsigned v0, const void* sbase, unsigned lds) {
    asm volatile("s_mov_b32 m0, %2\n\ts_nop 0\n\t"
                 "global_load_lds_dwordx4 %0, %1"
                 ::"v"(v0), "s"(sbase), "s"(lds) : "memory");
}
DEVI void touch(unsigned voff, const void* sbase, unsigned lds) {
    asm volatile("s_mov_b32 m0, %2\n\ts_nop 0\n\t"
                 "global_load_lds_dword %0, %1" TOUCH_POL
                 ::"v"(voff), "s"(sbase), "s"(lds) : "memory");
}
DEVI unsigned cvtpk(float lo, float hi) {
    f32x2 v = {lo, hi};
    return __builtin_bit_cast(unsigned, __builtin_convertvector(v, bf16x2));
}
DEVI float fma1(float a, float b, float c) {
    float r;
    asm("v_fma_f32 %0, %1, %2, %3" : "=v"(r) : "v"(a), "v"(b), "v"(c));
    return r;
}
DEVI u32x2 trread(lptr p) {
    return __builtin_bit_cast(u32x2, __builtin_amdgcn_ds_read_tr16_b64_v4i16((LAS v4i16*)p));
}
DEVI f32x4 mfma(u32x4 a, u32x4 b, f32x4 c) {
    return __builtin_amdgcn_mfma_f32_16x16x32_bf16(__builtin_bit_cast(bf16x8, a), __builtin_bit_cast(bf16x8, b), c, 0, 0, 0);
}
#define STAMP(k, i)
#define STAMPARG
#define STAMPPASS
#define LGBAR() do { __builtin_amdgcn_s_waitcnt(0xc07f); asm volatile("s_waitcnt lgkmcnt(0)\n\ts_barrier" ::: "memory"); } while (0)
template <int N>
DEVI void vmbar() { asm volatile("s_waitcnt vmcnt(%0)\n\ts_barrier" ::"n"(N) : "memory"); }
#define LSTAMP(n)
#define LSTK
#define LSTP(k)
#define TTA
#define TTP
template <int NS, int NA, int NB, int LA, class FA, class FB, class FH>
DEVI void loader_sched(FA&& ia, FB&& ib, FH&& hook LSTK) {
#define vmbar_(N) vmbar<N>()
    constexpr int NI = NA + NB, FULL = LA / NI;
    constexpr bool HALF = (LA % NI) != 0;
    static_assert(LA % NI == 0 || LA % NI == NA, "bad LA");
    ia(); ib();
    hook();
#pragma unroll 1
    for (int i = 1; i <= FULL; ++i) { ia(); ib(); }
    if (HALF) ia();
    asm volatile("s_waitcnt vmcnt(%0)" ::"n"(LA) : "memory");
    LSTAMP(41);
#pragma unroll 1
    for (int s = 0; s <= NS - 2 - FULL - (HALF ? 1 : 0); ++s) {
        vmbar_(LA);
        if (HALF) { ib(); ia(); } else { ia(); ib(); }
    }
    if (HALF) { vmbar_(LA); ib(); }
    if (FULL >= 3) vmbar_(3 * NI);
    if (FULL >= 2) vmbar_(2 * NI);
    vmbar_(NI);
    vmbar_(0);
}

constexpr int A_SLOT = 28 * 1024;
constexpr int A_RING = 5;
constexpr int A_TAB = A_SLOT * A_RING;
constexpr int K1_SCR = A_TAB + 12 * 1024;
constexpr int K1_LDS = K1_SCR + 256;
constexpr int B_SLOT = 36 * 1024;
constexpr int B_RING = 4;

constexpr int L_ROWS = 17;
#define OPQ(a, b) asm("" : "+s"(a), "+s"(b))
DEVI void warm(int r, int c, int lane, int xi, int xj, const unsigned short* x, const int* qw, const unsigned short* U,
               const unsigned short* V, unsigned scr) {
    const int eb = 320 * (xj ^ (xi & 1)), nbb = 768 * xj + 384 * (xi >> 1);
    const bool xr = (unsigned)(r - PA) < 5u;
    const int p = r < PA ? r : r - 5, t = r - PA;
    int oq = (eb + 64 * c) * (F_ * 4) + xi * 128 + p * 512;
    int ov = (2 * p + (xi & 1)) * (64 * F_ * 2) + nbb * 2 + (c - 5) * 128;
    int ou = (128 * t + 32 * xi) * (D_ * 2) + eb * 2 + (c - 8) * 256;
    int ox = (512 * xi + 128 * (c & 3) + 64 * xj) * (D_ * 2) + (c >> 2) * 128 + t * 256;
    OPQ(oq, ov);
    OPQ(ou, ox);
    const bool q = c < 5, u = xr && c >= 8;
    const char* bw = q ? (const char*)qw : (const char*)V;
    const char* bx = u ? (const char*)U : (const char*)x;
    int ow = q ? oq : ov, oy = u ? ou : ox;
    int sw = q ? F_ * 4 : F_ * 2, ex = (xr && (c & ~1) == 8) ? 128 : 0;
    OPQ(ow, oy);
    OPQ(bw, bx);
    OPQ(sw, ex);
    const unsigned voff = (lane & (u ? 31 : 63)) * (xr ? D_ * 2 : sw) + (lane >> 5) * ex;
    touch(voff, (xr ? bx : bw) + (unsigned)(xr ? oy : ow), scr);
}

DEVI u32x4 deq8(i32x4 q0, i32x4 q1, f32x4 s0, f32x4 z0, f32x4 s1, f32x4 z1) {
    float f[8];
#pragma unroll
    for (int j = 0; j < 4; ++j) {
        f[j] = fma1((float)q0[j], s0[j], z0[j]);
        f[4 + j] = fma1((float)q1[j], s1[j], z1[j]);
    }
    return u32x4{cvtpk(f[0], f[1]), cvtpk(f[2], f[3]), cvtpk(f[4], f[5]), cvtpk(f[6], f[7])};
}

DEVI void w2_loader(unsigned smb, int lw, int e0, int n0, const int* __restrict__ qw, const unsigned short* __restrict__ V,
                    const void* keep0, const void* keep1 TTA) {
    const int lane = threadIdx.x & 63;
    const unsigned vv0 = (lane >> 4) * (F_ * 2) + (((lane & 15) ^ ((lane >> 4) << 1)) << 4);
    const unsigned vv1 = (vv0 ^ 128) + (4 * F_ * 2 - 1024);
    const unsigned vv2 = vv0 + 2 * (4 * F_ * 2 - 1024), vv3 = vv1 + 2 * (4 * F_ * 2 - 1024);
    const unsigned va0 = (lane >> 3) * (F_ * 4) + (((lane & 7) ^ (lane >> 4) ^ ((lw & 1) << 2)) << 4);
    const unsigned va1 = (va0 ^ 64) + (8 * F_ * 4 - 1024), va2 = va0 + 2 * (8 * F_ * 4 - 1024);
    unsigned la = smb + lw * 3072, lb = smb + 12288 + lw * 4096;
    const unsigned lenda = la + A_RING * A_SLOT, lendb = lb + A_RING * A_SLOT;
    const char* pq = (const char*)qw + ((e0 + 24 * (lw & 1)) * (F_ * 4) + (lw >> 1) * 128);
    const char* pv = (const char*)V + (16 * lw * (F_ * 2) + n0 * 2);
    loader_sched<24, 4, 3, W_LA>(
        [&]() {
            glds4(vv0, vv1, vv2, vv3, pv, lb);
            pv += 64 * F_ * 2;
            lb += A_SLOT;
            if (lb == lendb) lb -= A_RING * A_SLOT;
        },
        [&]() {
            glds3(va0, va1, va2, pq, la);
            pq += 256;
            la += A_SLOT;
            if (la == lenda) la -= A_RING * A_SLOT;
        },
        [&]() { asm volatile("" ::"s"(keep0), "s"(keep1)); } LSTP(0));
}

DEVI void w2_compute(lptr sm, int wid, int e0, int n0, bool skip0, u32x4 tsc, u32x4 tzr,
                     char* __restrict__ w2f STAMPARG) {
    const int tid = threadIdx.x, lane = tid & 63;
    const int nh = wid & 1, kp = (wid >> 1) & 1, hs = wid >> 2;
    const int own = 2 * kp + hs;
    const int g = lane >> 4, r16 = lane & 15;
    if (wid < 3) {
        asm volatile("s_waitcnt vmcnt(0)" : "+v"(tsc), "+v"(tzr)::"memory");
        lptr tp = sm + A_TAB + tid * 64;
#pragma unroll
        for (int h = 0; h < 2; ++h) {
            f32x4 s, z;
            s[0] = __uint_as_float(tsc[2 * h] << 16);
            s[1] = __uint_as_float(tsc[2 * h] & 0xffff0000u);
            s[2] = __uint_as_float(tsc[2 * h + 1] << 16);
            s[3] = __uint_as_float(tsc[2 * h + 1] & 0xffff0000u);
            z[0] = -__uint_as_float(tzr[2 * h] << 16) * s[0];
            z[1] = -__uint_as_float(tzr[2 * h] & 0xffff0000u) * s[1];
            z[2] = -__uint_as_float(tzr[2 * h + 1] << 16) * s[2];
            z[3] = -__uint_as_float(tzr[2 * h + 1] & 0xffff0000u) * s[3];
            *(LAS f32x4*)(tp + h * 32) = s;
            *(LAS f32x4*)(tp + h * 32 + 16) = z;
        }
    }
    asm volatile("s_waitcnt lgkmcnt(0)" ::: "memory");
    STAMP(0, 2);
    const int sw = (r16 >> 1) & 7;
    const int oa0 = kp * 6144 + r16 * 128 + ((g ^ sw) << 4);
    const int oa1 = kp * 6144 + r16 * 128 + (((4 + g) ^ sw) << 4);
    const int q4 = r16 >> 2, pp = r16 & 3;
    const int krow = 32 * kp + 4 * g + q4;
    int ov[4];
#pragma unroll
    for (int T = 0; T < 4; ++T) ov[T] = 12288 + krow * 256 + (((4 * nh + ((T + own) & 3)) ^ (krow & 7)) << 5) + 8 * pp;
    lptr ptab = sm + A_TAB + (8 * kp + g) * 32 + hs * 512;

    f32x4 acc[3][4];
#pragma unroll
    for (int t = 0; t < 3; ++t)
#pragma unroll
        for (int T = 0; T < 4; ++T) acc[t][T] = f32x4{0.f, 0.f, 0.f, 0.f};

    int slotoff = hs * A_SLOT;
    u32x4 v[4], a0;
    i32x4 q10, q11, q20, q21;
    f32x4 s0, z0, s1, z1;
    auto rd = [&](bool bar2) {
        lptr b = sm + slotoff;
        lptr pa0 = b + oa0, pa1 = b + oa1;
        lptr p0 = b + ov[0], p1 = b + ov[1], p2 = b + ov[2], p3 = b + ov[3];
        SB();
        LGBAR();
        i32x4 q00 = *(LAS i32x4*)(pa0), q01 = *(LAS i32x4*)(pa1);
        s0 = *(LAS f32x4*)(ptab), z0 = *(LAS f32x4*)(ptab + 16);
        s1 = *(LAS f32x4*)(ptab + 128), z1 = *(LAS f32x4*)(ptab + 144);
        SB();
        q10 = *(LAS i32x4*)(pa0 + 2048), q11 = *(LAS i32x4*)(pa1 + 2048);
        {
            u32x2 lo = trread(p1), hi = trread(p1 + 4096);
            v[1] = u32x4{lo[0], lo[1], hi[0], hi[1]};
            lo = trread(p2), hi = trread(p2 + 4096);
            v[2] = u32x4{lo[0], lo[1], hi[0], hi[1]};
            lo = trread(p3), hi = trread(p3 + 4096);
            v[3] = u32x4{lo[0], lo[1], hi[0], hi[1]};
            lo = trread(p0), hi = trread(p0 + 4096);
            v[0] = u32x4{lo[0], lo[1], hi[0], hi[1]};
        }
        q20 = *(LAS i32x4*)(pa0 + 4096), q21 = *(LAS i32x4*)(pa1 + 4096);
        slotoff += 2 * A_SLOT;
        if (slotoff >= A_RING * A_SLOT) slotoff -= A_RING * A_SLOT;
        ptab += 1024;
        SB();
        if (bar2) LGBAR();
        SB();
        a0 = deq8(q00, q01, s0, z0, s1, z1);
    };
    if (hs) LGBAR();
#pragma unroll 1
    for (int p = 0; p < 11; ++p) {
        rd(true);
        SB();
        float f[8];
#pragma unroll
        for (int j = 0; j < 4; ++j) {
            acc[0][j] = mfma(a0, v[j], acc[0][j]);
            f[j] = fma1((float)q10[j], s0[j], z0[j]);
            f[4 + j] = fma1((float)q11[j], s1[j], z1[j]);
            SB();
        }
        u32x4 a1 = u32x4{cvtpk(f[0], f[1]), cvtpk(f[2], f[3]), cvtpk(f[4], f[5]), cvtpk(f[6], f[7])};
        SB();
#pragma unroll
        for (int j = 0; j < 4; ++j) {
            acc[1][j] = mfma(a1, v[j], acc[1][j]);
            f[j] = fma1((float)q20[j], s0[j], z0[j]);
            f[4 + j] = fma1((float)q21[j], s1[j], z1[j]);
            SB();
        }
        u32x4 a2 = u32x4{cvtpk(f[0], f[1]), cvtpk(f[2], f[3]), cvtpk(f[4], f[5]), cvtpk(f[6], f[7])};
        SB();
#pragma unroll
        for (int T = 0; T < 4; ++T) acc[2][T] = mfma(a2, v[T], acc[2][T]);
    }
    rd(!hs);
    lptr lb = sm + lane * 16;
    lptr pw[4];
#pragma unroll
    for (int T = 1; T < 4; ++T) pw[T] = lb + ((nh * 4 + ((own + T) & 3)) * 3 + (3 - T)) * 3072;
    {
        SB();
#pragma unroll
        for (int T = 1; T < 4; ++T) acc[0][T] = mfma(a0, v[T], acc[0][T]);
        u32x4 a1 = deq8(q10, q11, s0, z0, s1, z1);
        SB();
#pragma unroll
        for (int T = 1; T < 4; ++T) acc[1][T] = mfma(a1, v[T], acc[1][T]);
#pragma unroll
        for (int T = 1; T < 4; ++T) *(LAS f32x4*)(pw[T]) = acc[0][T];
        u32x4 a2 = deq8(q20, q21, s0, z0, s1, z1);
        SB();
#pragma unroll
        for (int T = 1; T < 4; ++T) acc[2][T] = mfma(a2, v[T], acc[2][T]);
#pragma unroll
        for (int T = 1; T < 4; ++T) *(LAS f32x4*)(pw[T] + 1024) = acc[1][T];
        SB();
        acc[0][0] = mfma(a0, v[0], acc[0][0]);
        acc[1][0] = mfma(a1, v[0], acc[1][0]);
        acc[2][0] = mfma(a2, v[0], acc[2][0]);
#pragma unroll
        for (int T = 1; T < 4; ++T) *(LAS f32x4*)(pw[T] + 2048) = acc[2][T];
    }
    const int u0 = e0 >> 4, odd = u0 & 1;
    const int n16 = (n0 >> 4) + 4 * nh + own;
    const int nb = n16 / 6, Tk = n16 % 6;
    char* o = w2f + ((size_t)((nb * 20 + (u0 >> 1)) * 6 + Tk)) * 1024 + lane * 16;
    lptr pr = lb + (nh * 4 + own) * 9216;
    char *o1 = o + (odd ? 6144 : 0), *o2 = o + (odd ? 8 : 6144);
    asm volatile("" : "+v"(o1), "+v"(o2), "+v"(pr));
    SB();
    LGBAR();
    f32x4 r[3][3];
#pragma unroll
    for (int i = 0; i < 3; ++i)
#pragma unroll
        for (int j = 0; j < 3; ++j) r[i][j] = *(LAS f32x4*)(pr + (i * 3 + j) * 1024);
    SB();
    u32x2 c[3];
#pragma unroll
    for (int j = 0; j < 3; ++j) {
        f32x4 m = ((acc[j][0] + r[0][j]) + r[1][j]) + r[2][j];
        c[j] = u32x2{cvtpk(m[0], m[1]), cvtpk(m[2], m[3])};
    }
    u32x4 full = odd ? u32x4{c[1][0], c[1][1], c[2][0], c[2][1]} : u32x4{c[0][0], c[0][1], c[1][0], c[1][1]};
    u32x2 half = odd ? c[0] : c[2];
    ST16(o1, full, WPOL);
    if (!skip0) ST8(o2, half, HPOL);
    asm volatile("s_waitcnt vmcnt(0)" ::: "memory");
}

DEVI void xr_loader(unsigned smb, int lw, int m0, int e0, const unsigned short* __restrict__ x, const unsigned short* __restrict__ U TTA) {
    const int lane = threadIdx.x & 63;
    unsigned vx[4], vu[5];
#pragma unroll
    for (int u = 0; u < 4; ++u) {
        int row = 8 * u + (lane >> 3);
        int c = (lane & 7) ^ ((row >> 1) & 7);
        vx[u] = (unsigned)((m0 + 32 * lw + row) * (D_ * 2) + c * 16 - u * 1024);
    }
#pragma unroll
    for (int u = 0; u < 5; ++u) {
        int q = (5 * lw + u) * 64 + lane;
        int k = q / 20, c = q % 20;
        c ^= 2 * ((k >> 3) & 1);
        vu[u] = (unsigned)(k * (D_ * 2) + e0 * 2 + c * 16 - (u & 3) * 1024);
    }
    const char* px = (const char*)x;
    const char* pu = (const char*)U;
    unsigned la = smb + lw * 4096, lb = smb + 16384 + lw * 5120;
    const unsigned lenda = la + B_RING * B_SLOT, lendb = lb + B_RING * B_SLOT;
    if (XBAR) asm volatile("s_barrier" ::: "memory");
    loader_sched<10, 4, 5, X_LA>(
        [&]() {
            glds4(vx[0], vx[1], vx[2], vx[3], px, la);
            px += 128;
            la += B_SLOT;
            if (la == lenda) la -= B_RING * B_SLOT;
        },
        [&]() {
            glds4(vu[0], vu[1], vu[2], vu[3], pu, lb);
            glds1(vu[4], pu, lb + 4096);
            pu += 64 * D_ * 2;
            lb += B_SLOT;
            if (lb == lendb) lb -= B_RING * B_SLOT;
        }, [&]() {} LSTP(0));
}

DEVI void xr_compute(lptr sm, int wid, int m0, int e0, char* __restrict__ xrf STAMPARG) {
    const int tid = threadIdx.x, lane = tid & 63;
    const int g = lane >> 4, r16 = lane & 15;
    const int wm = wid & 1, eh = (wid >> 1) & 1, kp = wid >> 2;
    if (XBAR) asm volatile("s_barrier" ::: "memory");
    const int sw = (r16 >> 1) & 7;
    const int oxa = wm * 8192 + kp * 4096 + r16 * 128 + (((4 * kp + g) ^ sw) << 4);
    const int oxb = oxa ^ 4096;
    const int q4 = r16 >> 2, pp = r16 & 3;
    const int ourow = 16384 + (32 * kp + 8 * g + q4) * 320 + 8 * pp;
    const int g1 = g & 1, dE = (eh ? -g1 : g1) * 32;
    const int ouE = ourow + 160 * eh + dE, ouO = ourow + 160 * eh - dE;

    f32x4 acc[4][5];
#pragma unroll
    for (int t = 0; t < 4; ++t)
#pragma unroll
        for (int T = 0; T < 5; ++T) acc[t][T] = f32x4{0.f, 0.f, 0.f, 0.f};

    u32x4 xa[4], xb[4], u[4], ua, ub;
    auto uld = [&](lptr pE, lptr pO, int T) {
        lptr p = ((T & 1) ? pO : pE) + 32 * T;
        u32x2 lo = trread(p), hi = trread(p + 4 * 320);
        return u32x4{lo[0], lo[1], hi[0], hi[1]};
    };
    auto step = [&](u32x4* xc, u32x4* xn, u32x4& uc, u32x4& un, lptr b) {
        lptr pa = b + oxa, pb = b + oxb, pE = b + ouE, pO = b + ouO;
#pragma unroll
        for (int T = 0; T < 4; ++T) {
            SB();
            acc[0][T] = mfma(u[T], xc[0], acc[0][T]);
            if (T == 0) xn[0] = *(LAS u32x4*)(pa);
            if (T == 1) xn[3] = *(LAS u32x4*)(pb + 2048);
            SB();
            acc[1][T] = mfma(u[T], xc[1], acc[1][T]);
            if (T == 0) xn[1] = *(LAS u32x4*)(pa + 2048);
            if (T == 1) un = uld(pE, pO, 4);
            SB();
            acc[2][T] = mfma(u[T], xc[2], acc[2][T]);
            if (T == 0) xn[2] = *(LAS u32x4*)(pb);
            SB();
            acc[3][T] = mfma(u[T], xc[3], acc[3][T]);
            u[T] = uld(pE, pO, T);
        }
        SB();
#pragma unroll
        for (int t = 0; t < 4; ++t) acc[t][4] = mfma(uc, xc[t], acc[t][4]);
        SB();
    };
    LGBAR();
    xa[0] = *(LAS u32x4*)(sm + oxa);
    xa[1] = *(LAS u32x4*)(sm + oxa + 2048);
    xa[2] = *(LAS u32x4*)(sm + oxb);
    xa[3] = *(LAS u32x4*)(sm + oxb + 2048);
#pragma unroll
    for (int T = 0; T < 4; ++T) u[T] = uld(sm + ouE, sm + ouO, T);
    ua = uld(sm + ouE, sm + ouO, 4);
    int slotoff = B_SLOT;
#pragma unroll 1
    for (int s = 0; s < 4; ++s) {
        LGBAR();
        step(xa, xb, ua, ub, sm + slotoff);
        slotoff = (slotoff == (B_RING - 1) * B_SLOT) ? 0 : slotoff + B_SLOT;
        LGBAR();
        step(xb, xa, ub, ua, sm + slotoff);
        slotoff = (slotoff == (B_RING - 1) * B_SLOT) ? 0 : slotoff + B_SLOT;
    }
    LGBAR();
    step(xa, xb, ua, ub, sm + slotoff);
#pragma unroll
    for (int t = 3; t >= 0; --t) {
#pragma unroll
        for (int T = 0; T < 4; ++T) acc[t][T] = mfma(u[T], xb[t], acc[t][T]);
        acc[t][4] = mfma(ub, xb[t], acc[t][4]);
    }
    {
        const int pr = wid & 3;
        lptr xs = sm + 2 * B_SLOT + lane * 16;
        lptr pw = xs + (pr * 2 + (kp ^ 1)) * 10240, prd = xs + (pr * 2 + kp) * 10240;
#pragma unroll
        for (int t = 0; t < 2; ++t)
#pragma unroll
            for (int T = 0; T < 5; ++T) *(LAS f32x4*)(pw + (t * 5 + T) * 1024) = acc[2 + t][T];
        LGBAR();
#pragma unroll
        for (int t = 0; t < 2; ++t)
#pragma unroll
            for (int T = 0; T < 5; ++T) acc[t][T] += *(LAS f32x4*)(prd + (t * 5 + T) * 1024);
    }
    {
        const int mb = (m0 >> 5) + 2 * wm + kp;
        const int ks0 = e0 >> 5;
        char* o = xrf + lane * 16;
#pragma unroll
        for (int t = 0; t < 2; ++t) {
#pragma unroll
            for (int j = 0; j < 2; ++j) {
                f32x4 a0 = eh ? acc[t][2 * j + 1] : acc[t][2 * j], a1 = eh ? acc[t][2 * j + 2] : acc[t][2 * j + 1];
                u32x4 v = {cvtpk(a0[0], a0[1]), cvtpk(a0[2], a0[3]), cvtpk(a1[0], a1[1]), cvtpk(a1[2], a1[3])};
                ST16(o + ((size_t)((mb * 20 + ks0 + 3 * eh + j) * 2 + t)) * 1024, v, XPOL);
            }
            f32x4 a = eh ? acc[t][0] : acc[t][4];
            u32x2 v = {cvtpk(a[0], a[1]), cvtpk(a[2], a[3])};
            ST8(o + ((size_t)((mb * 20 + ks0 + 2) * 2 + t)) * 1024 + 8 * eh, v, HPOL);
        }
    }
    STAMP(0, 10);
    asm volatile("s_waitcnt vmcnt(0)" ::: "memory");
}

__global__ __launch_bounds__(768, 3) void k1(const unsigned short* __restrict__ x, const int* __restrict__ qw,
                                             const unsigned short* __restrict__ sc, const unsigned short* __restrict__ zr,
                                             const unsigned short* __restrict__ U, const unsigned short* __restrict__ V,
                                             char* __restrict__ xrf, unsigned short* y STAMPARG) {
    __shared__ __attribute__((aligned(1024))) char smem[K1_LDS];
    lptr sm = (lptr)smem;
    unsigned smb = (unsigned)(uintptr_t)sm;
    u32x4 tsc, tzr;
    if (__builtin_amdgcn_readfirstlane(threadIdx.x) < 192 && (blockIdx.x >> 3) - 3u < 21u)
        asm volatile("global_load_dwordx4 %0, %2, %3\n\tglobal_load_dwordx4 %1, %2, %4"
                     : "=&v"(tsc), "=&v"(tzr) : "v"(threadIdx.x * 16), "s"(sc), "s"(zr) : "memory");
    if (blockIdx.x < 8 && __builtin_amdgcn_readfirstlane(threadIdx.x) >= 512) {
        const char* pc;
        int len, len2;
        asm volatile("s_getpc_b64 %0\n"
                     "quip_code_begin:\n\t"
                     "s_mov_b32 %1, quip_code_end-quip_code_begin\n\t"
                     "s_mov_b32 %2, quip_code_end2-quip_code_begin" : "=s"(pc), "=s"(len), "=s"(len2));
        len = len > len2 ? len : len2;
        const int off = ((int)threadIdx.x - 512) * 128;
        touch(off < len ? off : 0, pc, smb + K1_SCR);
    }
    const int xcd = blockIdx.x & 7, idx = blockIdx.x >> 3;
    const int wid = __builtin_amdgcn_readfirstlane(threadIdx.x >> 6);
    const int lane = threadIdx.x & 63;
    const int xi = xcd >> 1, xj = xcd & 1;
    const int hh = xj ^ (xi & 1);
    const int eb = 320 * hh, nbb = 768 * xj + 384 * (xi >> 1), mb = 512 * xi;
    const unsigned iw = idx - 3;
    if (iw >= 21u && wid < RX) {
#pragma unroll 1
        for (int i = TB + wid * TD; i > 0; --i) __builtin_amdgcn_s_sleep(1);
        warm(wid + P0, idx < 3 ? idx : idx - 21, lane, xi, xj, x, qw, U, V, smb + K1_SCR);
    }
    SB();
    if (__builtin_expect(iw < 21u, 1)) {
        const int tb = (iw * 37) >> 8, ta = iw - 7 * tb;
        const int e0w = eb + (ta < 6 ? 48 * ta : 272);
        const int n0w = nbb + 128 * tb;
        if (__builtin_expect(wid >= 8, 1)) {
            w2_loader(smb, wid - 8, e0w, n0w, qw, V, x, U TTP);
            if (iw == 0 && wid == 8) {
                const char* pc;
                int o2, len, len2;
                asm volatile("s_getpc_b64 %0\n"
                             "quip_here:\n\t"
                             "s_mov_b32 %1, quip_k2_begin-quip_here\n\t"
                             "s_mov_b32 %2, quip_code_end-quip_k2_begin\n\t"
                             "s_mov_b32 %3, quip_code_end2-quip_k2_begin" : "=s"(pc), "=s"(o2), "=s"(len), "=s"(len2));
                len = len > len2 ? len : len2;
                const int off = lane * 128;
                touch(off < len + 128 ? off : 0, pc + (o2 - 128), smb + K1_SCR);
                asm volatile("s_waitcnt vmcnt(0)" ::: "memory");
            }
            return;
        }
        STAMP(0, 0);
        w2_compute(sm, wid, e0w, n0w, ta == 6, tsc, tzr, xrf + S_ * D_ * 2 STAMPPASS);
        return;
    }
    STAMP(0, 0);
    if (idx >= 24) {
        const int i2 = idx - 24;
        const int m0 = mb + 128 * (i2 & 3);
        const int e0 = eb + 160 * (i2 >> 2);
        if (wid >= 8) xr_loader(smb, wid - 8, m0, e0, x, U TTP);
        else xr_compute(sm, wid, m0, e0, xrf STAMPPASS);
    } else {
        const unsigned w = wid * 3 + idx;
        if (w < 33) {
#pragma unroll 1
            for (int r = RX + P0 + w / 11; r < L_ROWS; r += 3) warm(r, w % 11, lane, xi, xj, x, qw, U, V, smb + K1_SCR);
        }
        asm volatile("s_waitcnt vmcnt(0)" ::: "memory");
        STAMP(0, 10);
    }
}

constexpr int C_SLOT = 28 * 1024;
constexpr int C_RING = 5;
constexpr int C_PITCH = 208;
struct CRegs {
    u32x4 x[2];
    u32x4 w[3];
};
__global__ __launch_bounds__(768, 3) void k2(const char* __restrict__ xrf, unsigned short* __restrict__ y STAMPARG) {
    __shared__ __attribute__((aligned(1024))) char smem[C_SLOT * C_RING + 256];
    asm volatile("quip_k2_begin:");
    lptr sm = (lptr)smem;
    unsigned smb = (unsigned)(uintptr_t)sm;
    const char* ws = xrf;
    unsigned short* yy = y;
    const int tid = threadIdx.x, lane = tid & 63;
    const int wid = __builtin_amdgcn_readfirstlane(tid >> 6);
    const int xcd = blockIdx.x & 7, idx = blockIdx.x >> 3;
    const int xi = xcd >> 1, xj = xcd & 1;
    const int m0 = 512 * xi + 128 * (idx & 3);
    const int nb = 8 * xj + (idx >> 2);
    const int g = lane >> 4, r16 = lane & 15;
    unsigned short *yp0, *yp1;
    lptr yl;
    auto epi_addr = [&]() {
        const int r = tid / 12, c = tid % 12;
        yl = sm + r * C_PITCH + c * 16;
        yp0 = yy + (size_t)(m0 + r) * F_ + nb * 96 + c * 8;
        yp1 = yp0 + 64 * F_;
        asm volatile("" : "+v"(yl), "+v"(yp0), "+v"(yp1));
    };
    if (__builtin_expect(wid >= 8, 1)) {
        const int lw = wid - 8;
        const int hh = xj ^ (xi & 1);
        unsigned vw = lane * 16 + (S_ * D_ * 2 + nb * (120 * 1024) + lw * 3072);
        unsigned vx = lane * 16 + ((m0 >> 5) + lw) * (40 * 1024);
        unsigned la = smb + lw * 4096, lb = smb + 16384 + lw * 3072;
        const unsigned lenda = la + C_RING * C_SLOT, lendb = lb + C_RING * C_SLOT;
        int sa = 5 * hh * 4096, sb = 5 * hh * 12288;
        asm volatile("" : "+v"(vw), "+v"(vx), "+s"(la), "+s"(lb), "+s"(sa), "+s"(sb));
        loader_sched<10, 4, 3, C_LA>(
            [&]() {
                glds4(vx, vx, vx, vx, ws + sa, la);
                la += C_SLOT;
                if (la == lenda) la -= C_RING * C_SLOT;
                sa = (sa == 9 * 4096) ? 0 : sa + 4096;
            },
            [&]() {
                glds3(vw, vw, vw, ws + sb, lb);
                lb += C_SLOT;
                if (lb == lendb) lb -= C_RING * C_SLOT;
                sb = (sb == 9 * 12288) ? 0 : sb + 12288;
            }, [&]() {} LSTP(1));
        SB();
        epi_addr();
    } else {
        epi_addr();
        STAMP(1, 0);
        const int wm = wid & 3, nh = wid >> 2;
        f32x4 acc[2][3];
#pragma unroll
        for (int t = 0; t < 2; ++t)
#pragma unroll
            for (int T = 0; T < 3; ++T) acc[t][T] = f32x4{0.f, 0.f, 0.f, 0.f};
        const int ox = wm * 4096 + lane * 16, ow = 16384 + nh * 3072 + lane * 16;
        CRegs Ra[2], Rb[2];
        auto step = [&](CRegs* cur, CRegs* nxt, lptr b) {
            lptr px = b + ox, pw = b + ow;
#pragma unroll
            for (int h = 0; h < 2; ++h) {
                SB();
                acc[0][0] = mfma(cur[h].w[0], cur[h].x[0], acc[0][0]);
                if (h == 0) { nxt[0].x[0] = *(LAS u32x4*)(px); nxt[0].w[0] = *(LAS u32x4*)(pw); }
                else nxt[1].w[1] = *(LAS u32x4*)(pw + 6144 + 1024);
                SB();
                acc[1][0] = mfma(cur[h].w[0], cur[h].x[1], acc[1][0]);
                if (h == 0) { nxt[0].x[1] = *(LAS u32x4*)(px + 1024); nxt[0].w[1] = *(LAS u32x4*)(pw + 1024); }
                else nxt[1].w[2] = *(LAS u32x4*)(pw + 6144 + 2048);
                SB();
                acc[0][1] = mfma(cur[h].w[1], cur[h].x[0], acc[0][1]);
                if (h == 0) nxt[0].w[2] = *(LAS u32x4*)(pw + 2048);
                SB();
                acc[1][1] = mfma(cur[h].w[1], cur[h].x[1], acc[1][1]);
                if (h == 0) nxt[1].x[0] = *(LAS u32x4*)(px + 2048);
                SB();
                acc[0][2] = mfma(cur[h].w[2], cur[h].x[0], acc[0][2]);
                if (h == 0) nxt[1].w[0] = *(LAS u32x4*)(pw + 6144);
                SB();
                acc[1][2] = mfma(cur[h].w[2], cur[h].x[1], acc[1][2]);
                if (h == 0) nxt[1].x[1] = *(LAS u32x4*)(px + 3072);
                SB();
            }
        };
        LGBAR();
#pragma unroll
        for (int h = 0; h < 2; ++h) {
            Ra[h].x[0] = *(LAS u32x4*)(sm + ox + h * 2048);
            Ra[h].x[1] = *(LAS u32x4*)(sm + ox + h * 2048 + 1024);
#pragma unroll
            for (int T = 0; T < 3; ++T) Ra[h].w[T] = *(LAS u32x4*)(sm + ow + h * 6144 + T * 1024);
        }
        int slotoff = C_SLOT;
#pragma unroll 1
        for (int s = 0; s < 4; ++s) {
            LGBAR();
            step(Ra, Rb, sm + slotoff);
            slotoff = (slotoff == (C_RING - 1) * C_SLOT) ? 0 : slotoff + C_SLOT;
            LGBAR();
            step(Rb, Ra, sm + slotoff);
            slotoff = (slotoff == (C_RING - 1) * C_SLOT) ? 0 : slotoff + C_SLOT;
        }
        lptr pst = sm + (32 * wm + r16) * C_PITCH + (48 * nh + 4 * g) * 2;
#pragma unroll
        for (int h = 0; h < 2; ++h)
#pragma unroll
            for (int T = 0; T < 3; ++T) {
                acc[0][T] = mfma(Ra[h].w[T], Ra[h].x[0], acc[0][T]);
                acc[1][T] = mfma(Ra[h].w[T], Ra[h].x[1], acc[1][T]);
            }
        {
            lptr px = sm + slotoff + ox, pw = sm + slotoff + ow;
            asm volatile("" : "+v"(px), "+v"(pw), "+v"(pst));
            SB();
            LGBAR();
#pragma unroll
            for (int h = 0; h < 2; ++h) {
                Rb[h].x[0] = *(LAS u32x4*)(px + h * 2048);
                SB();
                Rb[h].w[0] = *(LAS u32x4*)(pw + h * 6144);
                SB();
                Rb[h].x[1] = *(LAS u32x4*)(px + h * 2048 + 1024);
                SB();
                Rb[h].w[1] = *(LAS u32x4*)(pw + h * 6144 + 1024);
                SB();
                Rb[h].w[2] = *(LAS u32x4*)(pw + h * 6144 + 2048);
                SB();
            }
            SB();
        }
#pragma unroll
        for (int h = 0; h < 2; ++h)
#pragma unroll
            for (int T = 0; T < 3; ++T) {
                acc[0][T] = mfma(Rb[h].w[T], Rb[h].x[0], acc[0][T]);
                acc[1][T] = mfma(Rb[h].w[T], Rb[h].x[1], acc[1][T]);
                SB();
            }
#pragma unroll
        for (int T = 0; T < 3; ++T)
#pragma unroll
            for (int t = 0; t < 2; ++t) {
                f32x4 a = acc[t][T];
                u32x2 v = {cvtpk(a[0], a[1]), cvtpk(a[2], a[3])};
                *(LAS u32x2*)(pst + 16 * t * C_PITCH + 32 * T) = v;
                SB();
            }
        asm volatile("quip_code_end2:");
    }
    LGBAR();
    {
        u32x4 v0 = *(LAS u32x4*)(yl), v1 = *(LAS u32x4*)(yl + 64 * C_PITCH);
        SB();
        ST16(yp0, v0, YPOL);
        ST16(yp1, v1, YPOL);
    }
    STAMP(1, 10);
    asm volatile("quip_code_end:");
}

extern "C" void kernel_launch(void* const* d_in, const int* in_sizes, int n_in, void* d_out, int out_size, void* d_ws,
                              size_t ws_size, hipStream_t stream) {
    const unsigned short* x = (const unsigned short*)d_in[0];
    const int* qw = (const int*)d_in[1];
    const unsigned short* sc = (const unsigned short*)d_in[2];
    const unsigned short* zr = (const unsigned short*)d_in[3];
    const unsigned short* U = (const unsigned short*)d_in[4];
    const unsigned short* V = (const unsigned short*)d_in[5];
    char* xrf = (char*)d_ws;
    k1<<<256, 768, 0, stream>>>(x, qw, sc, zr, U, V, xrf, (unsigned short*)d_out STAMPPASS);
    k2<<<256, 768, 0, stream>>>(xrf, (unsigned short*)d_out STAMPPASS);
}
```
